```python
import torch

def _softmax(x, axis=-1):
    return torch.softmax(x, dim=axis)
def _gelu(x):
    return torch.nn.functional.gelu(x, approximate="tanh")
def _silu(x):
    return torch.nn.functional.silu(x)
def _sigmoid(x):
    return torch.sigmoid(x)
def _layernorm(x, axis=-1, eps=1e-5):
    m = x.mean(dim=axis, keepdim=True)
    v = x.var(dim=axis, keepdim=True, unbiased=False)
    return (x - m) / torch.sqrt(v + eps)
def _scatter_add(tgt, idx, src, axis=0):
    return tgt.clone().index_add_(axis, idx, src)
def _segment_sum(src, seg, n):
    out = torch.zeros((n,) + src.shape[1:], dtype=src.dtype)
    return out.index_add_(0, seg, src)
def _pad(x, pw):
    flat = tuple(v for pair in reversed(pw) for v in pair)
    return torch.nn.functional.pad(x, flat)

kernel_name = 'standalone__geglu_ffn__sampling'

def setup_inputs():

    _w = (lambda a: (lambda t: t.to(torch.bfloat16) if t.is_floating_point() else t)(torch.as_tensor(a)))
    B, S, D, F = 8, 8, 1024, 3584
    return {
        "x": _w((lambda *s: torch.randn(*s, dtype=torch.bfloat16) * 0.05)(B, S, D)),
        "w_gate": _w((lambda *s: torch.randn(*s, dtype=torch.bfloat16) * 0.05)(D, F)),
        "w_up": _w((lambda *s: torch.randn(*s, dtype=torch.bfloat16) * 0.05)(D, F)),
        "w_down": _w((lambda *s: torch.randn(*s, dtype=torch.bfloat16) * 0.05)(F, D)),
    }

def reference(x, w_gate, w_up, w_down):
    return (_gelu(x @ w_gate) * (x @ w_up)) @ w_down
```

```cpp
#include <hip/hip_runtime.h>
#include <hip/hip_bf16.h>
#include <stdint.h>

typedef short bf16x8 __attribute__((ext_vector_type(8)));
typedef float f32x4 __attribute__((ext_vector_type(4)));
typedef unsigned v4u __attribute__((ext_vector_type(4)));

constexpr int M = 64, D = 1024, F = 3584;

__device__ __forceinline__ float gelu_tanh(float g) {
    float z = 0.7978845608028654f * (g + 0.044715f * g * g * g);
    return g / (1.0f + __expf(-2.0f * z));
}
__device__ __forceinline__ unsigned short f2bf(float f) {
    __hip_bfloat16 b = __float2bfloat16(f);
    return *(unsigned short*)&b;
}

__global__ void __launch_bounds__(256) k1(const unsigned short* __restrict__ x, const unsigned short* __restrict__ wg,
                   const unsigned short* __restrict__ wu, unsigned short* __restrict__ h) {
    __shared__ f32x4 red[4][8][64];
    int lane = threadIdx.x & 63, w = threadIdx.x >> 6;
    int n0 = blockIdx.x * 16;
    f32x4 acc[8];
    for (int i = 0; i < 8; i++) acc[i] = f32x4{0,0,0,0};
    int r = lane & 15, g = lane >> 4;
    for (int ks = 0; ks < 8; ks++) {
        int k = w * 256 + ks * 32 + g * 8;
        bf16x8 bg, bu;
        for (int j = 0; j < 8; j++) { bg[j] = wg[(k + j) * F + n0 + r]; bu[j] = wu[(k + j) * F + n0 + r]; }
        for (int t = 0; t < 4; t++) {
            bf16x8 a = *(const bf16x8*)(x + (t * 16 + r) * D + k);
            acc[t] = __builtin_amdgcn_mfma_f32_16x16x32_bf16(a, bg, acc[t], 0, 0, 0);
            acc[4 + t] = __builtin_amdgcn_mfma_f32_16x16x32_bf16(a, bu, acc[4 + t], 0, 0, 0);
        }
    }
    for (int i = 0; i < 8; i++) red[w][i][lane] = acc[i];
    __syncthreads();
    f32x4 gg = red[0][w][lane] + red[1][w][lane] + red[2][w][lane] + red[3][w][lane];
    f32x4 uu = red[0][4 + w][lane] + red[1][4 + w][lane] + red[2][4 + w][lane] + red[3][4 + w][lane];
    for (int j = 0; j < 4; j++) {
        int tok = w * 16 + g * 4 + j;
        h[tok * F + n0 + r] = f2bf(gelu_tanh(gg[j]) * uu[j]);
    }
}

__global__ void __launch_bounds__(256) k2(const unsigned short* __restrict__ h, const unsigned short* __restrict__ wd,
                   unsigned short* __restrict__ out) {
    __shared__ f32x4 red[4][64];
    int lane = threadIdx.x & 63, w = threadIdx.x >> 6;
    int n0 = (blockIdx.x >> 2) * 16, t0 = (blockIdx.x & 3) * 16;
    f32x4 acc = {0,0,0,0};
    int r = lane & 15, g = lane >> 4;
    for (int ks = 0; ks < 28; ks++) {
        int k = w * 896 + ks * 32 + g * 8;
        bf16x8 b;
        for (int j = 0; j < 8; j++) b[j] = wd[(k + j) * D + n0 + r];
        bf16x8 a = *(const bf16x8*)(h + (t0 + r) * F + k);
        acc = __builtin_amdgcn_mfma_f32_16x16x32_bf16(a, b, acc, 0, 0, 0);
    }
    red[w][lane] = acc;
    __syncthreads();
    if (w == 0) {
        f32x4 s = red[0][lane] + red[1][lane] + red[2][lane] + red[3][lane];
        for (int j = 0; j < 4; j++) out[(t0 + g * 4 + j) * D + n0 + r] = f2bf(s[j]);
    }
}

extern "C" void kernel_launch(void* const* d_in, const int* in_sizes, int n_in,
                              void* d_out, int out_size, void* d_ws, size_t ws_size,
                              hipStream_t stream) {
    const unsigned short* x = (const unsigned short*)d_in[0];
    const unsigned short* wg = (const unsigned short*)d_in[1];
    const unsigned short* wu = (const unsigned short*)d_in[2];
    const unsigned short* wd = (const unsigned short*)d_in[3];
    unsigned short* h = (unsigned short*)d_ws;
    k1<<<224, 256, 0, stream>>>(x, wg, wu, h);
    k2<<<256, 256, 0, stream>>>(h, wd, (unsigned short*)d_out);
}
```

```cpp
#include <hip/hip_runtime.h>
#include <hip/hip_bf16.h>
#include <stdint.h>

typedef short bf16x8 __attribute__((ext_vector_type(8)));
typedef float f32x4 __attribute__((ext_vector_type(4)));
typedef unsigned v4u __attribute__((ext_vector_type(4)));

constexpr int M = 64, D = 1024, F = 3584;

__device__ __forceinline__ float gelu_tanh(float g) {
    float z = 0.7978845608028654f * (g + 0.044715f * g * g * g);
    return g / (1.0f + __expf(-2.0f * z));
}
__device__ __forceinline__ unsigned short f2bf(float f) {
    __hip_bfloat16 b = __float2bfloat16(f);
    return *(unsigned short*)&b;
}

__global__ void __launch_bounds__(256) k1(const unsigned short* __restrict__ x, const unsigned short* __restrict__ wg,
                   const unsigned short* __restrict__ wu, unsigned short* __restrict__ h) {
    __shared__ f32x4 red[4][8][64];
    int lane = threadIdx.x & 63, w = threadIdx.x >> 6;
    int n0 = blockIdx.x * 16;
    f32x4 acc[8];
    for (int i = 0; i < 8; i++) acc[i] = f32x4{0,0,0,0};
    int r = lane & 15, g = lane >> 4;
    for (int ks = 0; ks < 8; ks++) {
        int k = w * 256 + ks * 32 + g * 8;
        bf16x8 bg, bu;
        for (int j = 0; j < 8; j++) { bg[j] = wg[(k + j) * F + n0 + r]; bu[j] = wu[(k + j) * F + n0 + r]; }
        for (int t = 0; t < 4; t++) {
            bf16x8 a = *(const bf16x8*)(x + (t * 16 + r) * D + k);
            acc[t] = __builtin_amdgcn_mfma_f32_16x16x32_bf16(a, bg, acc[t], 0, 0, 0);
            acc[4 + t] = __builtin_amdgcn_mfma_f32_16x16x32_bf16(a, bu, acc[4 + t], 0, 0, 0);
        }
    }
    for (int i = 0; i < 8; i++) red[w][i][lane] = acc[i];
    __syncthreads();
    f32x4 gg = red[0][w][lane] + red[1][w][lane] + red[2][w][lane] + red[3][w][lane];
    f32x4 uu = red[0][4 + w][lane] + red[1][4 + w][lane] + red[2][4 + w][lane] + red[3][4 + w][lane];
    for (int j = 0; j < 4; j++) {
        int tok = w * 16 + g * 4 + j;
        h[tok * F + n0 + r] = f2bf(gelu_tanh(gg[j]) * uu[j]);
    }
}

__global__ void __launch_bounds__(256) k2(const unsigned short* __restrict__ h, const unsigned short* __restrict__ wd,
                   unsigned short* __restrict__ out) {
    __shared__ f32x4 red[4][64];
    int lane = threadIdx.x & 63, w = threadIdx.x >> 6;
    int n0 = (blockIdx.x >> 2) * 16, t0 = (blockIdx.x & 3) * 16;
    f32x4 acc = {0,0,0,0};
    int r = lane & 15, g = lane >> 4;
    for (int ks = 0; ks < 28; ks++) {
        int k = w * 896 + ks * 32 + g * 8;
        bf16x8 b;
        for (int j = 0; j < 8; j++) b[j] = wd[(k + j) * D + n0 + r];
        bf16x8 a = *(const bf16x8*)(h + (t0 + r) * F + k);
        acc = __builtin_amdgcn_mfma_f32_16x16x32_bf16(a, b, acc, 0, 0, 0);
    }
    red[w][lane] = acc;
    __syncthreads();
    if (w == 0) {
        f32x4 s = red[0][lane] + red[1][lane] + red[2][lane] + red[3][lane];
        for (int j = 0; j < 4; j++) out[(t0 + g * 4 + j) * D + n0 + r] = f2bf(s[j]);
    }
}


#define GAS __attribute__((address_space(1)))
#define LAS __attribute__((address_space(3)))
template<int NWAVES, int ROT, int AUX0, int AUX1, int NQ, int ICACHE>
__global__ void __launch_bounds__(NWAVES*64) kprobe(const char* __restrict__ wg, const char* __restrict__ wu) {
  extern __shared__ __attribute__((aligned(16))) char smem[];
  unsigned long long t0 = wall_clock64();
  const int lane = threadIdx.x & 63, w = __builtin_amdgcn_readfirstlane(threadIdx.x >> 6);
  const int b = blockIdx.x, xcd = b & 7, s = b >> 3;
  constexpr int PW = 64 / NWAVES;
  if (ICACHE) {
    #pragma unroll
    for (int i=0;i<ICACHE;i++) asm volatile("s_nop 0\n s_nop 0\n s_nop 0\n s_nop 0\n s_nop 0\n s_nop 0\n s_nop 0\n s_nop 0\n s_nop 0\n s_nop 0\n s_nop 0\n s_nop 0\n s_nop 0\n s_nop 0\n s_nop 0\n s_nop 0");
  } else
  if (s < 28) {
    const int hg = xcd*7 + (s>>2), tg = s & 3;
    const char* base = ((lane>>3)&1) ? wu : wg;
    const char* lp = base + (size_t)(lane>>4)*7168 + hg*128 + (lane&7)*16;
    #pragma unroll
    for (int q=0;q<NQ;q++) {
      int qq = ROT ? ((q + tg)&3) : q;
      #pragma unroll
      for (int i=0;i<PW;i++) {
        int row = qq*256 + (i*NWAVES + w)*4;
        const char* p = lp + (size_t)row*7168;
        if (q==0) __builtin_amdgcn_global_load_lds((const GAS void*)p, (LAS void*)(smem + ((q&1)*64 + i*NWAVES + w)*1024), 16, 0, AUX0);
        else __builtin_amdgcn_global_load_lds((const GAS void*)p, (LAS void*)(smem + ((q&1)*64 + i*NWAVES + w)*1024), 16, 0, AUX1);
      }
    }
    asm volatile("s_waitcnt vmcnt(0)" ::: "memory");
  }
  __builtin_amdgcn_sched_barrier(0);
  unsigned long long t1 = wall_clock64();
  unsigned long long tend = t1 + 20*(t1-t0);
  while (wall_clock64() < tend) __builtin_amdgcn_s_sleep(4);
}

extern "C" void kernel_launch(void* const* d_in, const int* in_sizes, int n_in,
                              void* d_out, int out_size, void* d_ws, size_t ws_size,
                              hipStream_t stream) {
    const unsigned short* x = (const unsigned short*)d_in[0];
    const unsigned short* wg = (const unsigned short*)d_in[1];
    const unsigned short* wu = (const unsigned short*)d_in[2];
    const unsigned short* wd = (const unsigned short*)d_in[3];
    unsigned short* h = (unsigned short*)d_ws;
    hipFuncSetAttribute((const void*)kprobe<8,1,0,0,1,1>, hipFuncAttributeMaxDynamicSharedMemorySize, 128*1024);
    kprobe<8,1,0,0,1,1><<<256,512,128*1024,stream>>>((const char*)wg,(const char*)wu);
    k1<<<224, 256, 0, stream>>>(x, wg, wu, h);
    k2<<<256, 256, 0, stream>>>(h, wd, (unsigned short*)d_out);
}
```

```cpp
#include <hip/hip_runtime.h>
#include <hip/hip_bf16.h>
#include <stdint.h>

#define GAS __attribute__((address_space(1)))
#define LAS __attribute__((address_space(3)))
typedef unsigned v4u __attribute__((ext_vector_type(4)));
typedef unsigned v2u __attribute__((ext_vector_type(2)));
typedef short s16x4 __attribute__((ext_vector_type(4)));
typedef short s16x8 __attribute__((ext_vector_type(8)));
typedef float f32x4 __attribute__((ext_vector_type(4)));
typedef float f32x16 __attribute__((ext_vector_type(16)));
typedef __bf16 bf16x2 __attribute__((ext_vector_type(2)));
typedef float f32x2 __attribute__((ext_vector_type(2)));

#define LOOKAHEAD 3
#define WD_TRIG 12
#define WD_AUX 0
#define FLAG_AT 3
#define H_MIN_TICKS 310
#define P_DELAY 0

constexpr int OFF_E = 0, OFF_G = 4096, OFF_H = 65536, OFF_P = 1 << 20, WS_USED = OFF_P + (2 << 20);
constexpr int SC1 = 16;
constexpr unsigned SPIN_CAP = 1u << 18;
constexpr int LDS_BYTES = 160 * 1024;

#define SBAR() __builtin_amdgcn_sched_barrier(0)
#define MEMFENCE() asm volatile("" ::: "memory")
#define MFMA32(a, b, c) __builtin_amdgcn_mfma_f32_32x32x16_bf16(a, b, c, 0, 0, 0)

#define STAMP(i) do {} while (0)
#define STAMP_OUT() do {} while (0)

__device__ __forceinline__ s16x4 tr_read(LAS char* p) {
    return __builtin_amdgcn_ds_read_tr16_b64_v4i16((LAS s16x4*)p);
}
__device__ __forceinline__ s16x8 cat8(s16x4 a, s16x4 b) {
    return __builtin_shufflevector(a, b, 0, 1, 2, 3, 4, 5, 6, 7);
}
__device__ __forceinline__ unsigned pack_bf16(float lo, float hi) {
    f32x2 v = {lo, hi};
    return __builtin_bit_cast(unsigned, __builtin_convertvector(v, bf16x2));
}
__device__ __forceinline__ unsigned next_tag(unsigned e) { return ((e & 0x7F7F7F7Fu) + 0x01010101u) ^ (e & 0x80808080u); }
__device__ __forceinline__ float geglu(float g, float u) {
    const float c0 = -2.0f * 0.7978845608028654f * 1.4426950408889634f;
    const float c1 = c0 * 0.044715f;
    float t = g * g;
    float e = __builtin_amdgcn_exp2f(g * __builtin_fmaf(t, c1, c0));
    return g * __builtin_amdgcn_rcpf(1.0f + e) * u;
}

__global__ void __launch_bounds__(512, 2)
geglu_ffn_kernel(const char* __restrict__ x, const char* __restrict__ wg, const char* __restrict__ wu,
                 const char* __restrict__ wd, char* out, char* ws) {
    extern __shared__ __attribute__((aligned(16))) char smem_raw[];
    LAS char* L = (LAS char*)smem_raw;
    const int lane = threadIdx.x & 63;
    const int w = __builtin_amdgcn_readfirstlane(threadIdx.x >> 6);
    const int xcd = blockIdx.x & 7, s = blockIdx.x >> 3;
    STAMP(0);

    if (w < 4) {
        if (s >= 18) {
            const int pi = s - 18, hg = xcd * 7 + (pi >> 1), th = pi & 1;
            {
                const __amdgpu_buffer_rsrc_t rx = __builtin_amdgcn_make_buffer_rsrc((void*)x, 0, 64 * 1024 * 2, 0x00020000);
                const int l0 = lane ^ (w >> 1);
                const int sb = th * 65536 + w * 1024;
#pragma unroll
                for (int i = 0; i < 16; i++)
                    __builtin_amdgcn_raw_ptr_buffer_load_lds(rx, L + (i * 4 + w) * 1024, 16, (l0 ^ ((2 * i) & 15)) * 16, sb + i * 4096, 0, 0);
            }
            const int mat = w & 1;
            const __amdgpu_buffer_rsrc_t rw = __builtin_amdgcn_make_buffer_rsrc((void*)(mat ? wu : wg), 0, 1024 * 3584 * 2, 0x00020000);
            const int r8 = lane >> 3, pos = lane & 7;
            const int segl = (pos >> 1) ^ (((r8 >> 1) & 1) | (mat << 1));
            const int voff = r8 * 7168 + segl * 32 + (pos & 1) * 16;
            const int sb = hg * 128 + (w >> 1) * 8 * 7168;
            LAS char* lb = L + 65536 + mat * 8192 + (w >> 1) * 1024;
            auto issue = [&](int c) {
#pragma unroll
                for (int i = 0; i < 4; i++)
                    __builtin_amdgcn_raw_ptr_buffer_load_lds(rw, lb + (c % 6) * 16384 + i * 2048, 16, voff, sb + (c * 64 + i * 16) * 7168, 0, 0);
            };
#pragma unroll
            for (int c = 0; c < LOOKAHEAD; c++) issue(c);
#pragma unroll
            for (int c = 0; c < 16; c++) {
                if (c + LOOKAHEAD < 16) issue(c + LOOKAHEAD);
                const int rem = (15 - c < LOOKAHEAD ? 15 - c : LOOKAHEAD) * 4;
                switch (rem) {
                    case 0: asm volatile("s_waitcnt vmcnt(0)\n\ts_barrier" ::: "memory"); break;
                    case 4: asm volatile("s_waitcnt vmcnt(4)\n\ts_barrier" ::: "memory"); break;
                    case 8: asm volatile("s_waitcnt vmcnt(8)\n\ts_barrier" ::: "memory"); break;
                    case 12: asm volatile("s_waitcnt vmcnt(12)\n\ts_barrier" ::: "memory"); break;
                    default: asm volatile("s_waitcnt vmcnt(16)\n\ts_barrier" ::: "memory"); break;
                }
            }
            STAMP(1);
        } else if (w == 0) {
            LAS char* scratch = L + 96 * 1024;
            if (s < 16) __builtin_amdgcn_global_load_lds((const GAS void*)(x + s * 8192 + lane * 128), scratch, 4, 0, 0);
            {
                const int lc = lane < 56 ? lane : 55;
                const int r = lc / 14, j = lc % 14;
                const char* lp = (j >= 7 ? wu - 896 : wg) + (r + 4 * s) * 7168 + xcd * 896 + j * 128;
#pragma unroll
                for (int i = 0; i < 14; i++)
                    __builtin_amdgcn_global_load_lds((const GAS void*)(lp + i * (18 * 4 * 7168)), scratch, 4, 0, 0);
                if (s < 4) __builtin_amdgcn_global_load_lds((const GAS void*)(lp + 14 * (18 * 4 * 7168)), scratch, 4, 0, 0);
            }
            const __amdgpu_buffer_rsrc_t rd = __builtin_amdgcn_make_buffer_rsrc((void*)wd, 0, 1024 * 3584 * 2, 0x00020000);
            if (s < 16) {
                const int r8 = lane >> 3, pos = lane & 7;
                const int segl = (pos >> 1) ^ (((r8 >> 1) & 1) << 1);
                const int voff = r8 * 2048 + segl * 32 + (pos & 1) * 16;
                const int sb = xcd * 448 * 2048 + s * 128;
#pragma unroll
                for (int p = 0; p < 56; p++)
                    __builtin_amdgcn_raw_ptr_buffer_load_lds(rd, L + p * 1024, 16, voff, sb + p * 8 * 2048, 0, WD_AUX);
                STAMP(1);
                asm volatile("s_waitcnt vmcnt(%0)\n\ts_barrier" ::"i"(WD_TRIG) : "memory");
                asm volatile("s_waitcnt vmcnt(0)\n\ts_barrier" ::: "memory");
                STAMP(2);
            } else {
                const int cg = ((3 - (int)((uintptr_t)wd >> 7)) & 7) + 8 * (s - 16);
#pragma unroll
                for (int i = 0; i < 4; i++)
                    __builtin_amdgcn_raw_ptr_buffer_load_lds(rd, scratch, 4, lane * 2048, (xcd * 448 + 192 + i * 64) * 2048 + cg * 128, 0, 0);
            }
        }
        STAMP_OUT();
        return;
    }

    const int cw = w - 4;
    const __amdgpu_buffer_rsrc_t rs = __builtin_amdgcn_make_buffer_rsrc((void*)ws, 0, WS_USED, 0x00020000);
    const int goff = OFF_G + (xcd * 32 + s) * 128;

    if (s >= 18) {
        const int pi = s - 18, hgl = pi >> 1, th = pi & 1;
        const int hoff = OFF_H + ((xcd * 2 + th) * 28 + hgl * 4 + cw) * 1024 + lane * 16;
        __builtin_amdgcn_raw_buffer_store_b128(v4u{~0u, ~0u, ~0u, ~0u}, rs, hoff, 0, SC1);
        unsigned E = __builtin_amdgcn_raw_buffer_load_b32(rs, OFF_E + (lane >> 6), 0, SC1);

        const int kh = lane >> 5, mat = (lane >> 4) & 1, q = (lane & 15) >> 2, p = lane & 3;
        const int pp = ((p & 1) << 1) | (p >> 1);
        LAS char* wb = L + 65536 + mat * 8192 + (8 * kh + q) * 128 + ((cw ^ ((q >> 1) | (mat << 1))) * 32) + pp * 8;
        const int tok = lane & 31;
        const int xb = tok * 2048 + ((kh ^ (tok & 15)) * 16);

        f32x16 acc;
#pragma unroll
        for (int i = 0; i < 16; i++) acc[i] = 0.f;
        s16x8 a[2][4], b[2][4];
#pragma unroll
        for (int c = 0; c <= 16; c++) {
            if (c < 16) {
                if (c == FLAG_AT) asm volatile("s_waitcnt vmcnt(0)" : "+v"(E)::"memory");
                asm volatile("s_waitcnt lgkmcnt(0)\n\ts_barrier" ::: "memory");
                SBAR();
                if (c == 0) STAMP(1);
#pragma unroll
                for (int t = 0; t < 4; t++) {
                    LAS char* pa = wb + (c % 6) * 16384 + t * 2048;
                    a[c & 1][t] = cat8(tr_read(pa), tr_read(pa + 512));
                    const int kt = c * 4 + t;
                    b[c & 1][t] = *(LAS s16x8*)(L + ((xb ^ ((kt & 7) * 32)) + (kt >> 3) * 256));
                }
                if (c == FLAG_AT && cw == 0 && lane == 0) __builtin_amdgcn_raw_buffer_store_b32(next_tag(E), rs, goff, 0, SC1);
                SBAR();
            }
            if (c > 0) {
#pragma unroll
                for (int t = 0; t < 4; t++) acc = MFMA32(a[(c - 1) & 1][t], b[(c - 1) & 1][t], acc);
                SBAR();
            }
        }
        STAMP(2);
        v4u hv;
#pragma unroll
        for (int i = 0; i < 4; i++) {
            unsigned d = pack_bf16(geglu(acc[2 * i], acc[8 + 2 * i]), geglu(acc[2 * i + 1], acc[9 + 2 * i]));
            hv[i] = d == ~0u ? 0x7FFF7FFFu : d;
        }
        __builtin_amdgcn_raw_buffer_store_b128(hv, rs, hoff, 0, SC1);
        STAMP(3);
        STAMP_OUT();
        return;
    }

    if (s >= 16) {
        if (blockIdx.x == 128 && cw == 0) {
            const unsigned T = next_tag(__builtin_amdgcn_raw_buffer_load_b32(rs, OFF_E, 0, SC1));
            const int fo = OFF_G + ((lane & 30) == 16 ? (lane & 32) : lane) * 128;
            for (unsigned spin = 0; spin < SPIN_CAP; spin++) {
                __builtin_amdgcn_s_sleep(32);
                bool ok = true;
#pragma unroll
                for (int i = 0; i < 4; i++) ok &= __builtin_amdgcn_raw_buffer_load_b32(rs, fo, i * 8192, SC1) == T;
                MEMFENCE();
                if (__all(ok)) {
                    if (lane == 0) __builtin_amdgcn_raw_buffer_store_b32(T, rs, OFF_E, 0, SC1);
                    break;
                }
            }
            STAMP(1);
        }
        STAMP_OUT();
        return;
    }

    const int cg = s;
    volatile LAS unsigned* tagw = (volatile LAS unsigned*)(L + 100 * 1024);
    if (cw == 0) {
        const unsigned long long t0 = __builtin_amdgcn_s_memrealtime();
        const unsigned T = next_tag(__builtin_amdgcn_raw_buffer_load_b32(rs, OFF_E, 0, SC1));
        if (lane == 0) {
            __builtin_amdgcn_raw_buffer_store_b32(T, rs, goff, 0, SC1);
            *tagw = T;
        }
        const int fo = OFF_G + (xcd * 32 + 18 + (lane < 14 ? lane : 13)) * 128;
        for (unsigned spin = 0; spin < SPIN_CAP; spin++) {
            __builtin_amdgcn_s_sleep(16);
            const unsigned g = __builtin_amdgcn_raw_buffer_load_b32(rs, fo, 0, SC1);
            MEMFENCE();
            if (__all(g == T)) break;
        }
        STAMP(1);
        while (__builtin_amdgcn_s_memrealtime() - t0 < H_MIN_TICKS) __builtin_amdgcn_s_sleep(1);
    }
    const int th = cw & 1, k2 = cw >> 1;
    const int hoff = OFF_H + ((xcd * 2 + th) * 28 + k2 * 14) * 1024;
    asm volatile("s_waitcnt lgkmcnt(0)\n\ts_barrier" ::: "memory");
    STAMP(2);
    v4u hf[14];
#pragma unroll
    for (int i = 0; i < 14; i++) hf[i] = __builtin_amdgcn_raw_buffer_load_b128(rs, lane * 16, hoff + i * 1024, SC1);
    SBAR();
    asm volatile("s_barrier" ::: "memory");
    SBAR();
    s16x8 af[2][14];
    {
        const int kh = lane >> 5, blk = (lane >> 4) & 1, q = (lane & 15) >> 2, p = lane & 3;
        LAS char* ab = L + (k2 * 224 + 8 * kh + q) * 128 + p * 8;
#pragma unroll
        for (int i = 0; i < 14; i++)
#pragma unroll
            for (int ch = 0; ch < 2; ch++) {
                LAS char* pa = ab + i * 2048 + (((2 * (ch ^ k2) + blk) ^ (q & 2)) * 32);
                af[ch][i] = cat8(tr_read(pa), tr_read(pa + 512));
            }
    }
    const unsigned T = __builtin_amdgcn_readfirstlane(*tagw);
    SBAR();
    STAMP(3);
    f32x16 acc[2];
    for (unsigned spin = 0;; spin++) {
#pragma unroll
        for (int i = 0; i < 16; i++) acc[0][i] = acc[1][i] = 0.f;
        unsigned mx = 0;
#pragma unroll
        for (int i = 0; i < 14; i++) {
            const s16x8 bfrag = __builtin_bit_cast(s16x8, hf[i]);
            acc[0] = MFMA32(af[0][i], bfrag, acc[0]);
            acc[1] = MFMA32(af[1][i], bfrag, acc[1]);
            mx = max(max(mx, max(hf[i].x, hf[i].y)), max(hf[i].z, hf[i].w));
        }
        if (!__any(mx == ~0u) || spin >= SPIN_CAP) break;
        MEMFENCE();
#pragma unroll
        for (int i = 0; i < 14; i++) hf[i] = __builtin_amdgcn_raw_buffer_load_b128(rs, lane * 16, hoff + i * 1024, SC1);
    }
    STAMP(4);
    const f32x16 keep = acc[0];
    {
        const f32x16 send = acc[1];
        LAS f32x4* ex = (LAS f32x4*)(L + 65536);
#pragma unroll
        for (int Q = 0; Q < 4; Q++) ex[(cw * 4 + Q) * 64 + lane] = f32x4{send[4 * Q], send[4 * Q + 1], send[4 * Q + 2], send[4 * Q + 3]};
        asm volatile("s_waitcnt lgkmcnt(0)\n\ts_barrier" ::: "memory");
        const int dst = th * 4 + ((lane & 31) >> 3);
        const int po = OFF_P + ((cg * 8 + dst) * 8 + xcd) * 2048 + (8 * k2 + (lane >> 5)) * 128 + (lane & 7) * 16;
#pragma unroll
        for (int Q = 0; Q < 4; Q++) {
            const f32x4 o = ex[((cw ^ 2) * 4 + Q) * 64 + lane];
            v4u pv;
#pragma unroll
            for (int j = 0; j < 4; j++)
                pv[j] = (__builtin_bit_cast(unsigned, keep[4 * Q + j] + o[j]) & 0xFFFFFF00u) | ((T >> (8 * j)) & 0xFFu);
            __builtin_amdgcn_raw_buffer_store_b128(pv, rs, po + Q * 256, 0, SC1);
        }
    }
    STAMP(5);
    if (P_DELAY) __builtin_amdgcn_s_sleep(P_DELAY);
    {
        const int po = ((cg * 8 + xcd) * 8 + (lane >> 5)) * 2048 + cw * 512 + (lane & 31) * 16;
        v4u pr[4];
        for (unsigned spin = 0;; spin++) {
            MEMFENCE();
#pragma unroll
            for (int i = 0; i < 4; i++) pr[i] = __builtin_amdgcn_raw_buffer_load_b128(rs, po + i * 4096, OFF_P, SC1);
            unsigned bad = 0;
#pragma unroll
            for (int i = 0; i < 4; i++) {
                const unsigned lo = __builtin_amdgcn_perm(pr[i].y, pr[i].x, 0x0C0C0400u);
                const unsigned hi = __builtin_amdgcn_perm(pr[i].w, pr[i].z, 0x04000C0Cu);
                bad |= (lo | hi) ^ T;
            }
            if (!__any(bad != 0) || spin >= SPIN_CAP) break;
        }
        STAMP(6);
        unsigned sum[4];
#pragma unroll
        for (int j = 0; j < 4; j++) {
            float v = __uint_as_float(pr[0][j] & 0xFFFFFF00u);
#pragma unroll
            for (int i = 1; i < 4; i++) v += __uint_as_float(pr[i][j] & 0xFFFFFF00u);
            sum[j] = __float_as_uint(v);
        }
        const auto s02 = __builtin_amdgcn_permlane32_swap(sum[0], sum[2], false, false);
        const auto s13 = __builtin_amdgcn_permlane32_swap(sum[1], sum[3], false, false);
        const unsigned a0 = s02[0], a1 = s02[1], b0 = s13[0], b1 = s13[1];
        const unsigned o = pack_bf16(__uint_as_float(a0) + __uint_as_float(a1), __uint_as_float(b0) + __uint_as_float(b1));
        *(GAS unsigned*)(out + (xcd * 8 + (lane & 7)) * 2048 + cg * 128 + (cw * 4 + ((lane & 31) >> 3)) * 8 + (lane >> 5) * 4) = o;
    }
    STAMP(7);
    STAMP_OUT();
}

extern "C" void kernel_launch(void* const* d_in, const int* in_sizes, int n_in, void* d_out, int out_size, void* d_ws,
                              size_t ws_size, hipStream_t stream) {
    (void)hipFuncSetAttribute((const void*)geglu_ffn_kernel, hipFuncAttributeMaxDynamicSharedMemorySize, LDS_BYTES);
    geglu_ffn_kernel<<<256, 512, LDS_BYTES, stream>>>((const char*)d_in[0], (const char*)d_in[1], (const char*)d_in[2],
                                                       (const char*)d_in[3], (char*)d_out, (char*)d_ws);
}
```

```cpp
#define PROFILE
#define AMP_ROLE 0
#define AMP_IDX 1
#include <hip/hip_runtime.h>
#include <hip/hip_bf16.h>
#include <stdint.h>

#define GAS __attribute__((address_space(1)))
#define LAS __attribute__((address_space(3)))
typedef unsigned v4u __attribute__((ext_vector_type(4)));
typedef unsigned v2u __attribute__((ext_vector_type(2)));
typedef short s16x4 __attribute__((ext_vector_type(4)));
typedef short s16x8 __attribute__((ext_vector_type(8)));
typedef float f32x4 __attribute__((ext_vector_type(4)));
typedef float f32x16 __attribute__((ext_vector_type(16)));
typedef __bf16 bf16x2 __attribute__((ext_vector_type(2)));
typedef float f32x2 __attribute__((ext_vector_type(2)));

#define LOOKAHEAD 3
#define WD_TRIG 12
#define WD_AUX 0
#define FLAG_AT 3
#define H_MIN_TICKS 310
#define P_DELAY 0

constexpr int OFF_E = 0, OFF_G = 4096, OFF_H = 65536, OFF_P = 1 << 20, WS_USED = OFF_P + (2 << 20);
constexpr int SC1 = 16;
constexpr unsigned SPIN_CAP = 1u << 18;
constexpr int LDS_BYTES = 160 * 1024;

#define SBAR() __builtin_amdgcn_sched_barrier(0)
#define MEMFENCE() asm volatile("" ::: "memory")
#define MFMA32(a, b, c) __builtin_amdgcn_mfma_f32_32x32x16_bf16(a, b, c, 0, 0, 0)

#define STAMP(i) do { SBAR(); stamps[i] = wall_clock64(); SBAR(); } while (0)
#define STAMP_OUT() do { const int role_ = (s >= 18 ? 0 : (s < 16 ? 1 : 9)) + (w < 4 ? 2 : 0); if (role_ == AMP_ROLE && stamps[AMP_IDX]) { const unsigned long long e_ = wall_clock64() + 20 * (stamps[AMP_IDX] - stamps[0]); while (wall_clock64() < e_) __builtin_amdgcn_s_sleep(4); } } while (0)

__device__ __forceinline__ s16x4 tr_read(LAS char* p) {
    return __builtin_amdgcn_ds_read_tr16_b64_v4i16((LAS s16x4*)p);
}
__device__ __forceinline__ s16x8 cat8(s16x4 a, s16x4 b) {
    return __builtin_shufflevector(a, b, 0, 1, 2, 3, 4, 5, 6, 7);
}
__device__ __forceinline__ unsigned pack_bf16(float lo, float hi) {
    f32x2 v = {lo, hi};
    return __builtin_bit_cast(unsigned, __builtin_convertvector(v, bf16x2));
}
__device__ __forceinline__ unsigned next_tag(unsigned e) { return ((e & 0x7F7F7F7Fu) + 0x01010101u) ^ (e & 0x80808080u); }
__device__ __forceinline__ float geglu(float g, float u) {
    const float c0 = -2.0f * 0.7978845608028654f * 1.4426950408889634f;
    const float c1 = c0 * 0.044715f;
    float t = g * g;
    float e = __builtin_amdgcn_exp2f(g * __builtin_fmaf(t, c1, c0));
    return g * __builtin_amdgcn_rcpf(1.0f + e) * u;
}

__global__ void __launch_bounds__(512, 2)
geglu_ffn_kernel(const char* __restrict__ x, const char* __restrict__ wg, const char* __restrict__ wu,
                 const char* __restrict__ wd, char* out, char* ws) {
    extern __shared__ __attribute__((aligned(16))) char smem_raw[];
    LAS char* L = (LAS char*)smem_raw;
    const int lane = threadIdx.x & 63;
    const int w = __builtin_amdgcn_readfirstlane(threadIdx.x >> 6);
    const int xcd = blockIdx.x & 7, s = blockIdx.x >> 3;
    unsigned long long stamps[10] = {0, 0, 0, 0, 0, 0, 0, 0, 0, 0};
    STAMP(0);

    if (w < 4) {
        if (s >= 18) {
            const int pi = s - 18, hg = xcd * 7 + (pi >> 1), th = pi & 1;
            {
                const __amdgpu_buffer_rsrc_t rx = __builtin_amdgcn_make_buffer_rsrc((void*)x, 0, 64 * 1024 * 2, 0x00020000);
                const int l0 = lane ^ (w >> 1);
                const int sb = th * 65536 + w * 1024;
#pragma unroll
                for (int i = 0; i < 16; i++)
                    __builtin_amdgcn_raw_ptr_buffer_load_lds(rx, L + (i * 4 + w) * 1024, 16, (l0 ^ ((2 * i) & 15)) * 16, sb + i * 4096, 0, 0);
            }
            const int mat = w & 1;
            const __amdgpu_buffer_rsrc_t rw = __builtin_amdgcn_make_buffer_rsrc((void*)(mat ? wu : wg), 0, 1024 * 3584 * 2, 0x00020000);
            const int r8 = lane >> 3, pos = lane & 7;
            const int segl = (pos >> 1) ^ (((r8 >> 1) & 1) | (mat << 1));
            const int voff = r8 * 7168 + segl * 32 + (pos & 1) * 16;
            const int sb = hg * 128 + (w >> 1) * 8 * 7168;
            LAS char* lb = L + 65536 + mat * 8192 + (w >> 1) * 1024;
            auto issue = [&](int c) {
#pragma unroll
                for (int i = 0; i < 4; i++)
                    __builtin_amdgcn_raw_ptr_buffer_load_lds(rw, lb + (c % 6) * 16384 + i * 2048, 16, voff, sb + (c * 64 + i * 16) * 7168, 0, 0);
            };
#pragma unroll
            for (int c = 0; c < LOOKAHEAD; c++) issue(c);
#pragma unroll
            for (int c = 0; c < 16; c++) {
                if (c + LOOKAHEAD < 16) issue(c + LOOKAHEAD);
                const int rem = (15 - c < LOOKAHEAD ? 15 - c : LOOKAHEAD) * 4;
                switch (rem) {
                    case 0: asm volatile("s_waitcnt vmcnt(0)\n\ts_barrier" ::: "memory"); break;
                    case 4: asm volatile("s_waitcnt vmcnt(4)\n\ts_barrier" ::: "memory"); break;
                    case 8: asm volatile("s_waitcnt vmcnt(8)\n\ts_barrier" ::: "memory"); break;
                    case 12: asm volatile("s_waitcnt vmcnt(12)\n\ts_barrier" ::: "memory"); break;
                    default: asm volatile("s_waitcnt vmcnt(16)\n\ts_barrier" ::: "memory"); break;
                }
            }
            STAMP(1);
        } else if (w == 0) {
            LAS char* scratch = L + 96 * 1024;
            if (s < 16) __builtin_amdgcn_global_load_lds((const GAS void*)(x + s * 8192 + lane * 128), scratch, 4, 0, 0);
            {
                const int lc = lane < 56 ? lane : 55;
                const int r = lc / 14, j = lc % 14;
                const char* lp = (j >= 7 ? wu - 896 : wg) + (r + 4 * s) * 7168 + xcd * 896 + j * 128;
#pragma unroll
                for (int i = 0; i < 14; i++)
                    __builtin_amdgcn_global_load_lds((const GAS void*)(lp + i * (18 * 4 * 7168)), scratch, 4, 0, 0);
                if (s < 4) __builtin_amdgcn_global_load_lds((const GAS void*)(lp + 14 * (18 * 4 * 7168)), scratch, 4, 0, 0);
            }
            const __amdgpu_buffer_rsrc_t rd = __builtin_amdgcn_make_buffer_rsrc((void*)wd, 0, 1024 * 3584 * 2, 0x00020000);
            if (s < 16) {
                const int r8 = lane >> 3, pos = lane & 7;
                const int segl = (pos >> 1) ^ (((r8 >> 1) & 1) << 1);
                const int voff = r8 * 2048 + segl * 32 + (pos & 1) * 16;
                const int sb = xcd * 448 * 2048 + s * 128;
#pragma unroll
                for (int p = 0; p < 56; p++)
                    __builtin_amdgcn_raw_ptr_buffer_load_lds(rd, L + p * 1024, 16, voff, sb + p * 8 * 2048, 0, WD_AUX);
                STAMP(1);
                asm volatile("s_waitcnt vmcnt(%0)\n\ts_barrier" ::"i"(WD_TRIG) : "memory");
                asm volatile("s_waitcnt vmcnt(0)\n\ts_barrier" ::: "memory");
                STAMP(2);
            } else {
                const int cg = ((3 - (int)((uintptr_t)wd >> 7)) & 7) + 8 * (s - 16);
#pragma unroll
                for (int i = 0; i < 4; i++)
                    __builtin_amdgcn_raw_ptr_buffer_load_lds(rd, scratch, 4, lane * 2048, (xcd * 448 + 192 + i * 64) * 2048 + cg * 128, 0, 0);
            }
        }
        STAMP_OUT();
        return;
    }

    const int cw = w - 4;
    const __amdgpu_buffer_rsrc_t rs = __builtin_amdgcn_make_buffer_rsrc((void*)ws, 0, WS_USED, 0x00020000);
    const int goff = OFF_G + (xcd * 32 + s) * 128;

    if (s >= 18) {
        const int pi = s - 18, hgl = pi >> 1, th = pi & 1;
        const int hoff = OFF_H + ((xcd * 2 + th) * 28 + hgl * 4 + cw) * 1024 + lane * 16;
        __builtin_amdgcn_raw_buffer_store_b128(v4u{~0u, ~0u, ~0u, ~0u}, rs, hoff, 0, SC1);
        unsigned E = __builtin_amdgcn_raw_buffer_load_b32(rs, OFF_E + (lane >> 6), 0, SC1);

        const int kh = lane >> 5, mat = (lane >> 4) & 1, q = (lane & 15) >> 2, p = lane & 3;
        const int pp = ((p & 1) << 1) | (p >> 1);
        LAS char* wb = L + 65536 + mat * 8192 + (8 * kh + q) * 128 + ((cw ^ ((q >> 1) | (mat << 1))) * 32) + pp * 8;
        const int tok = lane & 31;
        const int xb = tok * 2048 + ((kh ^ (tok & 15)) * 16);

        f32x16 acc;
#pragma unroll
        for (int i = 0; i < 16; i++) acc[i] = 0.f;
        s16x8 a[2][4], b[2][4];
#pragma unroll
        for (int c = 0; c <= 16; c++) {
            if (c < 16) {
                if (c == FLAG_AT) asm volatile("s_waitcnt vmcnt(0)" : "+v"(E)::"memory");
                asm volatile("s_waitcnt lgkmcnt(0)\n\ts_barrier" ::: "memory");
                SBAR();
                if (c == 0) STAMP(1);
#pragma unroll
                for (int t = 0; t < 4; t++) {
                    LAS char* pa = wb + (c % 6) * 16384 + t * 2048;
                    a[c & 1][t] = cat8(tr_read(pa), tr_read(pa + 512));
                    const int kt = c * 4 + t;
                    b[c & 1][t] = *(LAS s16x8*)(L + ((xb ^ ((kt & 7) * 32)) + (kt >> 3) * 256));
                }
                if (c == FLAG_AT && cw == 0 && lane == 0) __builtin_amdgcn_raw_buffer_store_b32(next_tag(E), rs, goff, 0, SC1);
                SBAR();
            }
            if (c > 0) {
#pragma unroll
                for (int t = 0; t < 4; t++) acc = MFMA32(a[(c - 1) & 1][t], b[(c - 1) & 1][t], acc);
                SBAR();
            }
        }
        STAMP(2);
        v4u hv;
#pragma unroll
        for (int i = 0; i < 4; i++) {
            unsigned d = pack_bf16(geglu(acc[2 * i], acc[8 + 2 * i]), geglu(acc[2 * i + 1], acc[9 + 2 * i]));
            hv[i] = d == ~0u ? 0x7FFF7FFFu : d;
        }
        __builtin_amdgcn_raw_buffer_store_b128(hv, rs, hoff, 0, SC1);
        STAMP(3);
        STAMP_OUT();
        return;
    }

    if (s >= 16) {
        if (blockIdx.x == 128 && cw == 0) {
            const unsigned T = next_tag(__builtin_amdgcn_raw_buffer_load_b32(rs, OFF_E, 0, SC1));
            const int fo = OFF_G + ((lane & 30) == 16 ? (lane & 32) : lane) * 128;
            for (unsigned spin = 0; spin < SPIN_CAP; spin++) {
                __builtin_amdgcn_s_sleep(32);
                bool ok = true;
#pragma unroll
                for (int i = 0; i < 4; i++) ok &= __builtin_amdgcn_raw_buffer_load_b32(rs, fo, i * 8192, SC1) == T;
                MEMFENCE();
                if (__all(ok)) {
                    if (lane == 0) __builtin_amdgcn_raw_buffer_store_b32(T, rs, OFF_E, 0, SC1);
                    break;
                }
            }
            STAMP(1);
        }
        STAMP_OUT();
        return;
    }

    const int cg = s;
    volatile LAS unsigned* tagw = (volatile LAS unsigned*)(L + 100 * 1024);
    if (cw == 0) {
        const unsigned long long t0 = __builtin_amdgcn_s_memrealtime();
        const unsigned T = next_tag(__builtin_amdgcn_raw_buffer_load_b32(rs, OFF_E, 0, SC1));
        if (lane == 0) {
            __builtin_amdgcn_raw_buffer_store_b32(T, rs, goff, 0, SC1);
            *tagw = T;
        }
        const int fo = OFF_G + (xcd * 32 + 18 + (lane < 14 ? lane : 13)) * 128;
        for (unsigned spin = 0; spin < SPIN_CAP; spin++) {
            __builtin_amdgcn_s_sleep(16);
            const unsigned g = __builtin_amdgcn_raw_buffer_load_b32(rs, fo, 0, SC1);
            MEMFENCE();
            if (__all(g == T)) break;
        }
        STAMP(1);
        while (__builtin_amdgcn_s_memrealtime() - t0 < H_MIN_TICKS) __builtin_amdgcn_s_sleep(1);
    }
    const int th = cw & 1, k2 = cw >> 1;
    const int hoff = OFF_H + ((xcd * 2 + th) * 28 + k2 * 14) * 1024;
    asm volatile("s_waitcnt lgkmcnt(0)\n\ts_barrier" ::: "memory");
    STAMP(2);
    v4u hf[14];
#pragma unroll
    for (int i = 0; i < 14; i++) hf[i] = __builtin_amdgcn_raw_buffer_load_b128(rs, lane * 16, hoff + i * 1024, SC1);
    SBAR();
    asm volatile("s_barrier" ::: "memory");
    SBAR();
    s16x8 af[2][14];
    {
        const int kh = lane >> 5, blk = (lane >> 4) & 1, q = (lane & 15) >> 2, p = lane & 3;
        LAS char* ab = L + (k2 * 224 + 8 * kh + q) * 128 + p * 8;
#pragma unroll
        for (int i = 0; i < 14; i++)
#pragma unroll
            for (int ch = 0; ch < 2; ch++) {
                LAS char* pa = ab + i * 2048 + (((2 * (ch ^ k2) + blk) ^ (q & 2)) * 32);
                af[ch][i] = cat8(tr_read(pa), tr_read(pa + 512));
            }
    }
    const unsigned T = __builtin_amdgcn_readfirstlane(*tagw);
    SBAR();
    STAMP(3);
    f32x16 acc[2];
    for (unsigned spin = 0;; spin++) {
#pragma unroll
        for (int i = 0; i < 16; i++) acc[0][i] = acc[1][i] = 0.f;
        unsigned mx = 0;
#pragma unroll
        for (int i = 0; i < 14; i++) {
            const s16x8 bfrag = __builtin_bit_cast(s16x8, hf[i]);
            acc[0] = MFMA32(af[0][i], bfrag, acc[0]);
            acc[1] = MFMA32(af[1][i], bfrag, acc[1]);
            mx = max(max(mx, max(hf[i].x, hf[i].y)), max(hf[i].z, hf[i].w));
        }
        if (!__any(mx == ~0u) || spin >= SPIN_CAP) break;
        MEMFENCE();
#pragma unroll
        for (int i = 0; i < 14; i++) hf[i] = __builtin_amdgcn_raw_buffer_load_b128(rs, lane * 16, hoff + i * 1024, SC1);
    }
    STAMP(4);
    const f32x16 keep = acc[0];
    {
        const f32x16 send = acc[1];
        LAS f32x4* ex = (LAS f32x4*)(L + 65536);
#pragma unroll
        for (int Q = 0; Q < 4; Q++) ex[(cw * 4 + Q) * 64 + lane] = f32x4{send[4 * Q], send[4 * Q + 1], send[4 * Q + 2], send[4 * Q + 3]};
        asm volatile("s_waitcnt lgkmcnt(0)\n\ts_barrier" ::: "memory");
        const int dst = th * 4 + ((lane & 31) >> 3);
        const int po = OFF_P + ((cg * 8 + dst) * 8 + xcd) * 2048 + (8 * k2 + (lane >> 5)) * 128 + (lane & 7) * 16;
#pragma unroll
        for (int Q = 0; Q < 4; Q++) {
            const f32x4 o = ex[((cw ^ 2) * 4 + Q) * 64 + lane];
            v4u pv;
#pragma unroll
            for (int j = 0; j < 4; j++)
                pv[j] = (__builtin_bit_cast(unsigned, keep[4 * Q + j] + o[j]) & 0xFFFFFF00u) | ((T >> (8 * j)) & 0xFFu);
            __builtin_amdgcn_raw_buffer_store_b128(pv, rs, po + Q * 256, 0, SC1);
        }
    }
    STAMP(5);
    if (P_DELAY) __builtin_amdgcn_s_sleep(P_DELAY);
    {
        const int po = ((cg * 8 + xcd) * 8 + (lane >> 5)) * 2048 + cw * 512 + (lane & 31) * 16;
        v4u pr[4];
        for (unsigned spin = 0;; spin++) {
            MEMFENCE();
#pragma unroll
            for (int i = 0; i < 4; i++) pr[i] = __builtin_amdgcn_raw_buffer_load_b128(rs, po + i * 4096, OFF_P, SC1);
            unsigned bad = 0;
#pragma unroll
            for (int i = 0; i < 4; i++) {
                const unsigned lo = __builtin_amdgcn_perm(pr[i].y, pr[i].x, 0x0C0C0400u);
                const unsigned hi = __builtin_amdgcn_perm(pr[i].w, pr[i].z, 0x04000C0Cu);
                bad |= (lo | hi) ^ T;
            }
            if (!__any(bad != 0) || spin >= SPIN_CAP) break;
        }
        STAMP(6);
        unsigned sum[4];
#pragma unroll
        for (int j = 0; j < 4; j++) {
            float v = __uint_as_float(pr[0][j] & 0xFFFFFF00u);
#pragma unroll
            for (int i = 1; i < 4; i++) v += __uint_as_float(pr[i][j] & 0xFFFFFF00u);
            sum[j] = __float_as_uint(v);
        }
        const auto s02 = __builtin_amdgcn_permlane32_swap(sum[0], sum[2], false, false);
        const auto s13 = __builtin_amdgcn_permlane32_swap(sum[1], sum[3], false, false);
        const unsigned a0 = s02[0], a1 = s02[1], b0 = s13[0], b1 = s13[1];
        const unsigned o = pack_bf16(__uint_as_float(a0) + __uint_as_float(a1), __uint_as_float(b0) + __uint_as_float(b1));
        *(GAS unsigned*)(out + (xcd * 8 + (lane & 7)) * 2048 + cg * 128 + (cw * 4 + ((lane & 31) >> 3)) * 8 + (lane >> 5) * 4) = o;
    }
    STAMP(7);
    STAMP_OUT();
}

extern "C" void kernel_launch(void* const* d_in, const int* in_sizes, int n_in, void* d_out, int out_size, void* d_ws,
                              size_t ws_size, hipStream_t stream) {
    (void)hipFuncSetAttribute((const void*)geglu_ffn_kernel, hipFuncAttributeMaxDynamicSharedMemorySize, LDS_BYTES);
    geglu_ffn_kernel<<<256, 512, LDS_BYTES, stream>>>((const char*)d_in[0], (const char*)d_in[1], (const char*)d_in[2],
                                                       (const char*)d_in[3], (char*)d_out, (char*)d_ws);
}
```

```cpp
#include <hip/hip_runtime.h>
#include <hip/hip_bf16.h>
#include <stdint.h>

constexpr int LDS_BYTES = 160 * 1024;

__global__ void __launch_bounds__(512, 2)
geglu_ffn_kernel(const char* x, const char* wg, const char* wu, const char* wd, char* out, char* ws) {
    asm volatile(
        "s_load_dwordx8 s[8:15], %[ka], 0x0\n"
        "v_readfirstlane_b32 s16, %[tid]\n"
        "s_and_b32 s18, %[b], 7\n"
        "v_bfe_u32 v2, %[tid], 3, 3\n"
        "v_and_b32 v3, 7, %[tid]\n"
        "s_mov_b32 s23, 0x20000\n"
        "s_mul_i32 s22, s23, 56\n"
        "s_cmpk_gt_u32 %[b], 0x7f\n"
        "s_cbranch_scc1 L_work%=\n"
        "s_cmpk_gt_u32 s16, 0x7f\n"
        "s_cbranch_scc1 L_end%=\n"
        "s_mov_b32 m0, 0\n"
        "s_getpc_b64 s[26:27]\n"
        "s_sub_u32 s17, %[b], s18\n"
        "s_lshl_b32 s19, s17, 6\n"
        "v_lshl_add_u32 v5, %[tid], 2, s19\n"
        "global_load_lds_dword v5, s[26:27]\n"
        "v_or_b32 v2, s17, v2\n"
        "v_min_u32 v3, 6, v3\n"
        "v_mul_u32_u24 v2, 56, v2\n"
        "s_mul_i32 s24, s18, 0x380\n"
        "v_add_lshl_u32 v2, v2, v3, 7\n"
        "v_lshlrev_b32 v4, 4, v5\n"
        "s_mul_i32 s25, s23, 7\n"
        "s_cmp_lt_u32 s16, 64\n"
        "s_waitcnt lgkmcnt(0)\n"
        "global_load_lds_dword v4, s[8:9]\n"
        "s_cselect_b64 s[20:21], s[10:11], s[12:13]\n"
        "s_and_b32 s21, s21, 0xffff\n"
        "buffer_load_dword v2, s[20:23], s24 offen lds\n"
        "s_add_u32 s24, s24, s25\n"
        "buffer_load_dword v2, s[20:23], s24 offen lds\n"
        "s_add_u32 s24, s24, s25\n"
        "buffer_load_dword v2, s[20:23], s24 offen lds\n"
        "s_add_u32 s24, s24, s25\n"
        "buffer_load_dword v2, s[20:23], s24 offen lds\n"
        "s_add_u32 s24, s24, s25\n"
        "buffer_load_dword v2, s[20:23], s24 offen lds\n"
        "s_add_u32 s24, s24, s25\n"
        "buffer_load_dword v2, s[20:23], s24 offen lds\n"
        "s_add_u32 s24, s24, s25\n"
        "buffer_load_dword v2, s[20:23], s24 offen lds\n"
        "s_add_u32 s24, s24, s25\n"
        "buffer_load_dword v2, s[20:23], s24 offen lds\n"
        "v_and_b32 v4, 63, %[tid]\n"
        "v_min_u32 v4, 55, v4\n"
        "v_lshlrev_b32 v4, 7, v4\n"
        "s_lshr_b32 s17, s17, 2\n"
        "s_lshr_b32 s19, s16, 6\n"
        "s_add_u32 s17, s17, s19\n"
        "s_mul_i32 s17, s17, 0x1c00\n"
        "s_mul_i32 s24, s18, s25\n"
        "s_add_u32 s24, s24, s17\n"
        "s_lshr_b32 s25, s25, 2\n"
        "s_mov_b32 s20, s14\n"
        "s_and_b32 s21, s15, 0xffff\n"
        "buffer_load_dword v4, s[20:23], s24 offen lds\n"
        "s_add_u32 s24, s24, s25\n"
        "buffer_load_dword v4, s[20:23], s24 offen lds\n"
        "s_add_u32 s24, s24, s25\n"
        "buffer_load_dword v4, s[20:23], s24 offen lds\n"
        "s_add_u32 s24, s24, s25\n"
        "buffer_load_dword v4, s[20:23], s24 offen lds\n"
        "L_end%=:\n"
        "s_endpgm\n"
        "L_work%=:\n"
        "s_cmpk_gt_u32 s16, 0xff\n"
        "s_cbranch_scc1 L_compute%=\n"
        "s_lshr_b32 s17, s16, 4\n"
        "v_and_b32 v4, 63, %[tid]\n"
        "v_xor_b32 v4, s17, v4\n"
        "s_lshl_b32 s19, s16, 7\n"
        "v_lshlrev_b32 v4, 4, v4\n"
        "s_mov_b32 m0, s19\n"
        "s_bfe_u32 s24, %[b], 0x10003\n"
        "s_lshl_b32 s24, s24, 16\n"
        "s_add_u32 s24, s24, s19\n"
        "s_mov_b32 s25, 0x8000\n"
        "s_movk_i32 s26, 0x8800\n"
        "s_mov_b32 s30, s23\n"
        "s_mov_b32 s31, s23\n"
        "v_xor_b32 v5, 16, v4\n"
        "v_xor_b32 v6, 32, v4\n"
        "v_xor_b32 v7, 48, v4\n"
        "s_waitcnt lgkmcnt(0)\n"
        "s_mov_b32 s28, s8\n"
        "s_and_b32 s29, s9, 0xffff\n"
        "buffer_load_dwordx4 v4, s[28:31], s24 offen lds\n"
        "buffer_load_dwordx4 v4, s[28:31], s24 offen offset:1024 lds\n"
        "s_add_u32 m0, m0, s25\n"
        "s_add_u32 s24, s24, s25\n"
        "buffer_load_dwordx4 v4, s[28:31], s24 offen lds\n"
        "buffer_load_dwordx4 v4, s[28:31], s24 offen offset:1024 lds\n"
        "s_add_u32 m0, m0, s26\n"
        "s_add_u32 s24, s24, s26\n"
        "buffer_load_dwordx4 v5, s[28:31], s24 offen lds\n"
        "buffer_load_dwordx4 v5, s[28:31], s24 offen offset:1024 lds\n"
        "s_add_u32 m0, m0, s25\n"
        "s_add_u32 s24, s24, s25\n"
        "buffer_load_dwordx4 v5, s[28:31], s24 offen lds\n"
        "buffer_load_dwordx4 v5, s[28:31], s24 offen offset:1024 lds\n"
        "s_add_u32 m0, m0, s26\n"
        "s_add_u32 s24, s24, s26\n"
        "buffer_load_dwordx4 v6, s[28:31], s24 offen lds\n"
        "buffer_load_dwordx4 v6, s[28:31], s24 offen offset:1024 lds\n"
        "s_add_u32 m0, m0, s25\n"
        "s_add_u32 s24, s24, s25\n"
        "buffer_load_dwordx4 v6, s[28:31], s24 offen lds\n"
        "buffer_load_dwordx4 v6, s[28:31], s24 offen offset:1024 lds\n"
        "s_add_u32 m0, m0, s26\n"
        "s_add_u32 s24, s24, s26\n"
        "buffer_load_dwordx4 v7, s[28:31], s24 offen lds\n"
        "buffer_load_dwordx4 v7, s[28:31], s24 offen offset:1024 lds\n"
        "s_add_u32 m0, m0, s25\n"
        "s_add_u32 s24, s24, s25\n"
        "buffer_load_dwordx4 v7, s[28:31], s24 offen lds\n"
        "buffer_load_dwordx4 v7, s[28:31], s24 offen offset:1024 lds\n"
        "s_load_dwordx2 s[28:29], %[ka], 0x28\n"
        "s_bitcmp1_b32 s16, 6\n"
        "s_cselect_b64 s[20:21], s[12:13], s[10:11]\n"
        "s_and_b32 s21, s21, 0xffff\n"
        "v_and_b32 v5, 2, v2\n"
        "v_lshlrev_b32 v3, 4, v3\n"
        "v_lshlrev_b32 v5, 4, v5\n"
        "s_and_b32 s17, s16, 64\n"
        "v_xor_b32 v3, v3, v5\n"
        "v_xor_b32 v3, s17, v3\n"
        "v_lshl_or_b32 v6, v2, 11, v3\n"
        "v_mul_u32_u24 v2, 0x1c00, v2\n"
        "v_add_u32 v2, v2, v3\n"
        "s_bfe_u32 s19, %[b], 0x30004\n"
        "s_min_u32 s19, s19, 6\n"
        "s_mul_i32 s24, s18, 7\n"
        "s_add_u32 s24, s24, s19\n"
        "s_lshl_b32 s24, s24, 7\n"
        "s_lshr_b32 s19, s16, 7\n"
        "s_mul_i32 s25, s19, 0xe000\n"
        "s_add_u32 s24, s24, s25\n"
        "s_lshl_b32 s19, s19, 10\n"
        "s_lshl_b32 s17, s17, 5\n"
        "s_add_u32 s19, s19, s17\n"
        "s_add_u32 m0, s19, 0x18000\n"
        "s_mov_b32 s25, 0x1c000\n"
        "s_movk_i32 s26, 0x1000\n"
        "s_movk_i32 s27, 13\n"
        "buffer_load_dwordx4 v2, s[20:23], s24 offen nt lds\n"
        "s_add_u32 m0, m0, s26\n"
        "s_add_u32 s24, s24, s25\n"
        "buffer_load_dwordx4 v2, s[20:23], s24 offen nt lds\n"
        "s_add_u32 m0, m0, s26\n"
        "s_add_u32 s24, s24, s25\n"
        "buffer_load_dwordx4 v2, s[20:23], s24 offen nt lds\n"
        "s_add_u32 m0, m0, s26\n"
        "s_add_u32 s24, s24, s25\n"
        "buffer_load_dwordx4 v2, s[20:23], s24 offen nt lds\n"
        "s_add_u32 m0, m0, s26\n"
        "s_add_u32 s24, s24, s25\n"
        "buffer_load_dwordx4 v2, s[20:23], s24 offen nt lds\n"
        "s_add_u32 m0, m0, s26\n"
        "s_add_u32 s24, s24, s25\n"
        "buffer_load_dwordx4 v2, s[20:23], s24 offen nt lds\n"
        "s_add_u32 m0, m0, s26\n"
        "s_add_u32 s24, s24, s25\n"
        "buffer_load_dwordx4 v2, s[20:23], s24 offen nt lds\n"
        "s_add_u32 m0, m0, s26\n"
        "s_add_u32 s24, s24, s25\n"
        "buffer_load_dwordx4 v2, s[20:23], s24 offen nt lds\n"
        "s_add_u32 m0, m0, s26\n"
        "s_add_u32 s24, s24, s25\n"
        "s_mov_b32 s36, s14\n"
        "s_and_b32 s37, s15, 0xffff\n"
        "s_mov_b32 s38, s22\n"
        "s_mov_b32 s39, s23\n"
        "s_mul_i32 s33, s18, 0xe0000\n"
        "s_bfe_u32 s19, %[b], 0x40003\n"
        "s_lshl_b32 s34, s19, 7\n"
        "s_add_u32 s33, s33, s34\n"
        "s_lshl_b32 s34, s16, 8\n"
        "s_add_u32 s33, s33, s34\n"
        "s_lshl_b32 s35, s16, 4\n"
        "s_add_u32 s35, s35, 0x10000\n"
        "s_lshl_b32 s34, s18, 11\n"
        "s_addk_i32 s34, 4096\n"
        "v_bfe_u32 v4, %[tid], 2, 4\n"
        "v_lshl_add_u32 v4, v4, 7, s34\n"
        "s_lshl_b32 s19, s19, 12\n"
        "s_lshr_b32 s34, s18, 1\n"
        "s_lshl_b32 s34, s34, 10\n"
        "s_add_u32 s19, s19, s34\n"
        "s_add_u32 s19, s19, 655360\n"
        "v_bfe_u32 v7, %[tid], 3, 3\n"
        "v_lshl_add_u32 v7, v7, 7, s19\n"
        "s_mov_b32 s34, 0x10000\n"
        "s_waitcnt lgkmcnt(0)\n"
        "s_load_dword s30, s[28:29], 0x0\n"
        "L_wl%=:\n"
        "buffer_load_dwordx4 v2, s[20:23], s24 offen nt lds\n"
        "s_add_u32 m0, m0, s26\n"
        "s_add_u32 s24, s24, s25\n"
        "buffer_load_dwordx4 v2, s[20:23], s24 offen nt lds\n"
        "s_add_u32 m0, m0, s26\n"
        "s_add_u32 s24, s24, s25\n"
        "buffer_load_dwordx4 v2, s[20:23], s24 offen nt lds\n"
        "s_add_u32 m0, m0, s26\n"
        "s_add_u32 s24, s24, s25\n"
        "buffer_load_dwordx4 v2, s[20:23], s24 offen nt lds\n"
        "s_add_u32 m0, m0, s26\n"
        "s_add_u32 s24, s24, s25\n"
        "s_cmp_ge_u32 m0, 0x28000\n"
        "s_cselect_b32 s17, 0x18000, 0\n"
        "s_sub_u32 m0, m0, s17\n"
        "s_sub_u32 s27, s27, 1\n"
        "s_waitcnt vmcnt(8) lgkmcnt(0)\n"
        "s_barrier\n"
        "s_cbranch_scc0 L_wl%=\n"
        "s_mov_b32 m0, s35\n"
        "s_mov_b32 s40, 262144\n"
        "buffer_load_dwordx4 v6, s[36:39], s33 offen nt lds\n"
        "s_add_u32 m0, m0, s26\n"
        "s_add_u32 s33, s33, s34\n"
        "buffer_load_dwordx4 v6, s[36:39], s33 offen nt lds\n"
        "s_add_u32 m0, m0, s26\n"
        "s_add_u32 s33, s33, s34\n"
        "buffer_load_dwordx4 v6, s[36:39], s33 offen nt lds\n"
        "s_add_u32 m0, m0, s26\n"
        "s_add_u32 s33, s33, s34\n"
        "buffer_load_dwordx4 v6, s[36:39], s33 offen nt lds\n"
        "s_add_u32 m0, m0, s26\n"
        "s_add_u32 s33, s33, s34\n"
        "s_waitcnt vmcnt(8)\n"
        "s_barrier\n"
        "buffer_load_dwordx4 v6, s[36:39], s33 offen nt lds\n"
        "s_add_u32 m0, m0, s26\n"
        "s_add_u32 s33, s33, s34\n"
        "buffer_load_dwordx4 v6, s[36:39], s33 offen nt lds\n"
        "s_add_u32 m0, m0, s26\n"
        "s_add_u32 s33, s33, s34\n"
        "buffer_load_dwordx4 v6, s[36:39], s33 offen nt lds\n"
        "s_add_u32 m0, m0, s26\n"
        "s_add_u32 s33, s33, s34\n"
        "buffer_load_dwordx4 v6, s[36:39], s33 offen nt lds\n"
        "s_add_u32 m0, m0, s26\n"
        "s_add_u32 s33, s33, s34\n"
        "s_waitcnt vmcnt(10)\n"
        "s_barrier\n"
        "s_waitcnt vmcnt(8)\n"
        "s_barrier\n"
        "s_cmp_eq_u32 s16, 0\n"
        "s_cbranch_scc1 L_chk%=\n"
        "s_waitcnt vmcnt(0)\n"
        "s_barrier\n"
        "buffer_load_dwordx4 v6, s[36:39], s33 offen nt lds\n"
        "s_add_u32 m0, m0, s26\n"
        "s_add_u32 s33, s33, s34\n"
        "buffer_load_dwordx4 v6, s[36:39], s33 offen nt lds\n"
        "s_add_u32 m0, m0, s26\n"
        "s_add_u32 s33, s33, s34\n"
        "buffer_load_dwordx4 v6, s[36:39], s33 offen nt lds\n"
        "s_add_u32 m0, m0, s26\n"
        "s_add_u32 s33, s33, s34\n"
        "buffer_load_dwordx4 v6, s[36:39], s33 offen nt lds\n"
        "s_add_u32 m0, m0, s26\n"
        "s_add_u32 s33, s33, s34\n"
        "buffer_load_dwordx4 v6, s[36:39], s33 offen nt lds\n"
        "s_add_u32 m0, m0, s26\n"
        "s_add_u32 s33, s33, s34\n"
        "buffer_load_dwordx4 v6, s[36:39], s33 offen nt lds\n"
        "s_add_u32 m0, m0, s26\n"
        "s_add_u32 s33, s33, s34\n"
        "s_barrier\n"
        "s_waitcnt vmcnt(0)\n"
        "s_barrier\n"
        "s_endpgm\n"
        "L_chk%=:\n"
        "s_and_b32 s31, s30, 0x7f7f7f7f\n"
        "s_add_u32 s31, s31, 0x01010101\n"
        "s_and_b32 s30, s30, 0x80808080\n"
        "s_xor_b32 s30, s30, s31\n"
        "s_sleep 5\n"
        "global_load_dword v5, v4, s[28:29] sc1\n"
        "s_waitcnt vmcnt(1)\n"
        "s_barrier\n"
        "buffer_load_dwordx4 v6, s[36:39], s33 offen nt lds\n"
        "s_add_u32 m0, m0, s26\n"
        "s_add_u32 s33, s33, s34\n"
        "buffer_load_dwordx4 v6, s[36:39], s33 offen nt lds\n"
        "s_add_u32 m0, m0, s26\n"
        "s_add_u32 s33, s33, s34\n"
        "buffer_load_dwordx4 v6, s[36:39], s33 offen nt lds\n"
        "s_add_u32 m0, m0, s26\n"
        "s_add_u32 s33, s33, s34\n"
        "buffer_load_dwordx4 v6, s[36:39], s33 offen nt lds\n"
        "s_add_u32 m0, m0, s26\n"
        "s_add_u32 s33, s33, s34\n"
        "buffer_load_dwordx4 v6, s[36:39], s33 offen nt lds\n"
        "s_add_u32 m0, m0, s26\n"
        "s_add_u32 s33, s33, s34\n"
        "buffer_load_dwordx4 v6, s[36:39], s33 offen nt lds\n"
        "s_add_u32 m0, m0, s26\n"
        "s_add_u32 s33, s33, s34\n"
        "s_waitcnt vmcnt(6)\n"
        "L_pl1%=:\n"
        "v_cmp_ne_u32 vcc, s30, v5\n"
        "s_cbranch_vccz L_pl1x%=\n"
        "s_sleep 2\n"
        "global_load_dword v5, v4, s[28:29] sc1\n"
        "s_sub_u32 s40, s40, 1\n"
        "s_waitcnt vmcnt(0)\n"
        "s_cbranch_scc0 L_pl1%=\n"
        "L_pl1x%=:\n"
        "s_barrier\n"
        "s_waitcnt vmcnt(0)\n"
        "s_barrier\n"
        "s_endpgm\n"
        "L_compute%=:\n"
        "s_waitcnt lgkmcnt(0)\n"
        "s_load_dwordx4 s[8:11], %[ka], 0x20\n"
        "v_and_b32 v1, 63, %[tid]\n"
        "s_bfe_u32 s17, s16, 0x20006\n"
        "s_mov_b32 s22, 3145728\n"
        "s_waitcnt lgkmcnt(0)\n"
        "s_load_dword s12, s[10:11], 0x0\n"
        "s_mov_b32 s20, s10\n"
        "s_and_b32 s21, s11, 0xffff\n"
        "s_mov_b32 s40, 262144\n"
        "v_and_b32 v12, 15, v1\n"
        "v_lshrrev_b32 v13, 4, v1\n"
        "v_bfe_u32 v14, v1, 4, 1\n"
        "v_lshrrev_b32 v15, 5, v1\n"
        "s_lshr_b32 s24, s17, 1\n"
        "s_lshl_b32 s25, s18, 1\n"
        "s_add_u32 s24, s24, s25\n"
        "s_mul_i32 s24, s24, 35840\n"
        "s_add_u32 s24, s24, 65536\n"
        "s_and_b32 s25, s17, 1\n"
        "s_lshl_b32 s26, s25, 8\n"
        "s_add_u32 s26, s26, s24\n"
        "s_lshl_b32 s25, s25, 7\n"
        "s_add_u32 s25, s25, s24\n"
        "s_addk_i32 s25, 0x400\n"
        "v_mul_u32_u24 v3, 1280, v15\n"
        "v_lshl_add_u32 v2, v14, 9, v3\n"
        "v_lshl_add_u32 v2, v12, 4, v2\n"
        "v_add_u32 v248, s26, v2\n"
        "v_lshl_add_u32 v3, v14, 6, v3\n"
        "v_lshl_add_u32 v3, v12, 2, v3\n"
        "v_add_u32 v249, s25, v3\n"
        "v_bfe_u32 v8, v1, 2, 2\n"
        "v_lshlrev_b32 v4, 10, v13\n"
        "v_lshl_or_b32 v4, v8, 7, v4\n"
        "v_lshrrev_b32 v8, 1, v8\n"
        "v_lshl_or_b32 v8, v14, 1, v8\n"
        "v_lshl_or_b32 v4, v8, 5, v4\n"
        "v_and_b32 v8, 3, v1\n"
        "v_lshl_or_b32 v4, v8, 3, v4\n"
        "v_add_u32 v250, 0x10000, v4\n"
        "s_and_b32 s24, %[b], 0x78\n"
        "s_lshl_b32 s25, s17, 1\n"
        "s_add_u32 s25, s25, s24\n"
        "s_lshl_b32 s25, s25, 14\n"
        "s_lshl_b32 s26, s18, 11\n"
        "s_add_u32 s25, s25, s26\n"
        "s_add_u32 s25, s25, 1048576\n"
        "v_bfe_u32 v8, v1, 3, 1\n"
        "v_lshlrev_b32 v8, 14, v8\n"
        "v_lshl_or_b32 v8, v13, 7, v8\n"
        "v_and_b32 v10, 7, v1\n"
        "v_lshl_or_b32 v8, v10, 4, v8\n"
        "v_add_u32 v251, s25, v8\n"
        "s_add_u32 s25, s24, s18\n"
        "s_lshl_b32 s25, s25, 14\n"
        "s_lshl_b32 s26, s17, 9\n"
        "s_add_u32 s25, s25, s26\n"
        "s_add_u32 s25, s25, 1048576\n"
        "v_and_b32 v14, 31, v1\n"
        "v_lshlrev_b32 v9, 4, v14\n"
        "v_lshl_or_b32 v9, v15, 11, v9\n"
        "v_add_u32 v252, s25, v9\n"
        "s_lshl_b32 s25, s18, 14\n"
        "s_lshl_b32 s26, s24, 4\n"
        "s_add_u32 s25, s25, s26\n"
        "s_lshl_b32 s26, s17, 5\n"
        "s_add_u32 s25, s25, s26\n"
        "v_lshlrev_b32 v10, 11, v10\n"
        "v_and_b32 v11, 24, v1\n"
        "v_or_b32 v10, v10, v11\n"
        "v_lshl_or_b32 v10, v15, 2, v10\n"
        "v_add_u32 v253, s25, v10\n"
        "s_lshl_b32 s25, s24, 9\n"
        "s_lshl_b32 s26, s17, 10\n"
        "s_add_u32 s25, s25, s26\n"
        "s_lshl_b32 s26, s18, 7\n"
        "s_add_u32 s25, s25, s26\n"
        "s_add_u32 s25, s25, 655360\n"
        "v_lshl_add_u32 v254, v14, 2, s25\n"
        "s_mov_b32 s41, 0x07060500\n"
        "s_mov_b32 s42, 0x07060501\n"
        "s_mov_b32 s43, 0x07060502\n"
        "s_mov_b32 s46, 0x07060503\n"
        "s_movk_i32 s52, 0x1000\n"
        "s_movk_i32 s53, 0x2000\n"
        "s_movk_i32 s54, 0x3000\n"
        "s_movk_i32 s33, 5120\n"
        "s_movk_i32 s34, 10240\n"
        "s_movk_i32 s35, 15360\n"
        "s_movk_i32 s36, 20480\n"
        "s_movk_i32 s37, 25600\n"
        "s_movk_i32 s38, 30720\n"
        "v_lshrrev_b32 v3, 5, v1\n"
        "v_bfe_u32 v12, v1, 4, 1\n"
        "v_bfe_u32 v13, v1, 2, 2\n"
        "v_lshlrev_b32 v2, 11, v12\n"
        "v_lshl_or_b32 v2, v3, 10, v2\n"
        "v_lshl_or_b32 v2, v13, 7, v2\n"
        "v_lshrrev_b32 v13, 1, v13\n"
        "v_lshl_or_b32 v13, v12, 1, v13\n"
        "v_xor_b32 v13, s17, v13\n"
        "v_lshl_or_b32 v2, v13, 5, v2\n"
        "v_bfrev_b32 v13, v1\n"
        "v_lshrrev_b32 v13, 30, v13\n"
        "v_lshl_or_b32 v2, v13, 3, v2\n"
        "v_add_u32 v2, 0x10000, v2\n"
        "v_and_b32 v13, 15, v1\n"
        "v_xor_b32 v4, v3, v13\n"
        "v_lshlrev_b32 v4, 4, v4\n"
        "v_lshl_or_b32 v4, v14, 11, v4\n"
        "v_xor_b32 v5, 32, v4\n"
        "v_xor_b32 v6, 64, v4\n"
        "v_xor_b32 v7, 96, v4\n"
        "v_xor_b32 v8, 128, v4\n"
        "v_xor_b32 v9, 160, v4\n"
        "v_xor_b32 v10, 192, v4\n"
        "v_xor_b32 v11, 224, v4\n"
        "s_bfe_u32 s24, %[b], 0x10003\n"
        "s_lshl_b32 s25, s18, 1\n"
        "s_add_u32 s24, s24, s25\n"
        "s_mul_i32 s24, s24, 28\n"
        "s_bfe_u32 s25, %[b], 0x30004\n"
        "s_lshl_b32 s25, s25, 2\n"
        "s_add_u32 s24, s24, s25\n"
        "s_add_u32 s24, s24, s17\n"
        "s_mul_i32 s24, s24, 1280\n"
        "s_add_u32 s24, s24, 65536\n"
        "v_lshlrev_b32 v13, 2, v13\n"
        "v_lshl_or_b32 v13, v3, 6, v13\n"
        "v_lshl_or_b32 v13, v12, 7, v13\n"
        "v_add_u32 v13, s24, v13\n"
        "v_lshl_add_u32 v12, v1, 4, s24\n"
        "s_lshl_b32 s25, s18, 11\n"
        "s_bfe_u32 s26, %[b], 0x40003\n"
        "s_lshl_b32 s26, s26, 7\n"
        "s_add_u32 s25, s25, s26\n"
        "s_addk_i32 s25, 4096\n"
        "v_lshl_add_u32 v15, v14, 2, s25\n"
        "s_mov_b32 s26, 0xbdd2d3e8\n"
        "s_mov_b32 s27, s26\n"
        "v_mov_b32 v94, 0xc0135761\n"
        "v_mov_b32 v95, v94\n"
        "v_mov_b32 v16, 0\n"
        "v_mov_b32 v17, 0\n"
        "v_mov_b32 v18, 0\n"
        "v_mov_b32 v19, 0\n"
        "v_mov_b32 v20, 0\n"
        "v_mov_b32 v21, 0\n"
        "v_mov_b32 v22, 0\n"
        "v_mov_b32 v23, 0\n"
        "v_mov_b32 v24, 0\n"
        "v_mov_b32 v25, 0\n"
        "v_mov_b32 v26, 0\n"
        "v_mov_b32 v27, 0\n"
        "v_mov_b32 v28, 0\n"
        "v_mov_b32 v29, 0\n"
        "v_mov_b32 v30, 0\n"
        "v_mov_b32 v31, 0\n"
        "v_mov_b32 v32, 0\n"
        "v_mov_b32 v33, 0\n"
        "v_mov_b32 v34, 0\n"
        "v_mov_b32 v35, 0\n"
        "v_mov_b32 v36, 0\n"
        "v_mov_b32 v37, 0\n"
        "v_mov_b32 v38, 0\n"
        "v_mov_b32 v39, 0\n"
        "v_mov_b32 v40, 0\n"
        "v_mov_b32 v41, 0\n"
        "v_mov_b32 v42, 0\n"
        "v_mov_b32 v43, 0\n"
        "v_mov_b32 v44, 0\n"
        "v_mov_b32 v45, 0\n"
        "v_mov_b32 v46, 0\n"
        "v_mov_b32 v47, 0\n"
        "v_mov_b32 v48, 0\n"
        "v_mov_b32 v49, 0\n"
        "v_mov_b32 v50, 0\n"
        "v_mov_b32 v51, 0\n"
        "v_mov_b32 v52, 0\n"
        "v_mov_b32 v53, 0\n"
        "v_mov_b32 v54, 0\n"
        "v_mov_b32 v55, 0\n"
        "v_mov_b32 v56, 0\n"
        "v_mov_b32 v57, 0\n"
        "v_mov_b32 v58, 0\n"
        "v_mov_b32 v59, 0\n"
        "v_mov_b32 v60, 0\n"
        "v_mov_b32 v61, 0\n"
        "v_mov_b32 v62, 0\n"
        "v_mov_b32 v63, 0\n"
        "s_movk_i32 s28, 6\n"
        "s_mov_b32 s29, 0x8000\n"
        "v_add_u32 v3, s29, v2\n"
        "s_waitcnt lgkmcnt(0)\n"
        "s_and_b32 s13, s12, 0x7f7f7f7f\n"
        "s_add_u32 s13, s13, 0x01010101\n"
        "s_and_b32 s12, s12, 0x80808080\n"
        "s_xor_b32 s12, s12, s13\n"
        "v_mov_b32 v255, s12\n"
        "s_and_b32 s48, s12, 0xff\n"
        "s_bfe_u32 s49, s12, 0x80008\n"
        "s_bfe_u32 s50, s12, 0x80010\n"
        "s_lshr_b32 s51, s12, 24\n"
        "L_p1l%=:\n"
        "s_barrier\n"
        "s_waitcnt lgkmcnt(9)\n"
        "v_mfma_f32_32x32x16_bf16 v[16:31], v[32:35], v[48:51], v[16:31]\n"
        "ds_read_b64_tr_b16 v[32:33], v3 offset:0\n"
        "ds_read_b64_tr_b16 v[34:35], v3 offset:512\n"
        "ds_read_b128 v[48:51], v4\n"
        "v_add_u32 v4, 0x100, v4\n"
        "s_waitcnt lgkmcnt(9)\n"
        "v_mfma_f32_32x32x16_bf16 v[16:31], v[36:39], v[52:55], v[16:31]\n"
        "ds_read_b64_tr_b16 v[36:37], v3 offset:4096\n"
        "ds_read_b64_tr_b16 v[38:39], v3 offset:4608\n"
        "ds_read_b128 v[52:55], v5\n"
        "v_add_u32 v5, 0x100, v5\n"
        "s_waitcnt lgkmcnt(9)\n"
        "v_mfma_f32_32x32x16_bf16 v[16:31], v[40:43], v[56:59], v[16:31]\n"
        "ds_read_b64_tr_b16 v[40:41], v3 offset:8192\n"
        "ds_read_b64_tr_b16 v[42:43], v3 offset:8704\n"
        "ds_read_b128 v[56:59], v6\n"
        "v_add_u32 v6, 0x100, v6\n"
        "s_waitcnt lgkmcnt(9)\n"
        "v_mfma_f32_32x32x16_bf16 v[16:31], v[44:47], v[60:63], v[16:31]\n"
        "ds_read_b64_tr_b16 v[44:45], v3 offset:12288\n"
        "ds_read_b64_tr_b16 v[46:47], v3 offset:12800\n"
        "ds_read_b128 v[60:63], v7\n"
        "v_add_u32 v7, 0x100, v7\n"
        "s_add_u32 s29, s29, 0x8000\n"
        "s_cmp_eq_u32 s29, 0x18000\n"
        "s_cselect_b32 s29, 0, s29\n"
        "s_barrier\n"
        "s_waitcnt lgkmcnt(9)\n"
        "v_mfma_f32_32x32x16_bf16 v[16:31], v[32:35], v[48:51], v[16:31]\n"
        "ds_read_b64_tr_b16 v[32:33], v3 offset:16384\n"
        "ds_read_b64_tr_b16 v[34:35], v3 offset:16896\n"
        "ds_read_b128 v[48:51], v8\n"
        "v_add_u32 v8, 0x100, v8\n"
        "s_waitcnt lgkmcnt(9)\n"
        "v_mfma_f32_32x32x16_bf16 v[16:31], v[36:39], v[52:55], v[16:31]\n"
        "ds_read_b64_tr_b16 v[36:37], v3 offset:20480\n"
        "ds_read_b64_tr_b16 v[38:39], v3 offset:20992\n"
        "ds_read_b128 v[52:55], v9\n"
        "v_add_u32 v9, 0x100, v9\n"
        "s_waitcnt lgkmcnt(9)\n"
        "v_mfma_f32_32x32x16_bf16 v[16:31], v[40:43], v[56:59], v[16:31]\n"
        "ds_read_b64_tr_b16 v[40:41], v3 offset:24576\n"
        "ds_read_b64_tr_b16 v[42:43], v3 offset:25088\n"
        "ds_read_b128 v[56:59], v10\n"
        "v_add_u32 v10, 0x100, v10\n"
        "s_waitcnt lgkmcnt(9)\n"
        "v_mfma_f32_32x32x16_bf16 v[16:31], v[44:47], v[60:63], v[16:31]\n"
        "ds_read_b64_tr_b16 v[44:45], v3 offset:28672\n"
        "ds_read_b64_tr_b16 v[46:47], v3 offset:29184\n"
        "ds_read_b128 v[60:63], v11\n"
        "v_add_u32 v11, 0x100, v11\n"
        "v_add_u32 v3, s29, v2\n"
        "s_sub_u32 s28, s28, 1\n"
        "s_cbranch_scc0 L_p1l%=\n"
        "s_cmp_lg_u32 s17, 0\n"
        "s_cbranch_scc1 L_p1f%=\n"
        "buffer_store_dword v255, v15, s[20:23], 0 offen sc1\n"
        "L_p1f%=:\n"
        "s_barrier\n"
        "s_waitcnt lgkmcnt(9)\n"
        "v_mfma_f32_32x32x16_bf16 v[16:31], v[32:35], v[48:51], v[16:31]\n"
        "ds_read_b64_tr_b16 v[32:33], v3 offset:0\n"
        "ds_read_b64_tr_b16 v[34:35], v3 offset:512\n"
        "ds_read_b128 v[48:51], v4\n"
        "s_waitcnt lgkmcnt(9)\n"
        "v_mfma_f32_32x32x16_bf16 v[16:31], v[36:39], v[52:55], v[16:31]\n"
        "ds_read_b64_tr_b16 v[36:37], v3 offset:4096\n"
        "ds_read_b64_tr_b16 v[38:39], v3 offset:4608\n"
        "ds_read_b128 v[52:55], v5\n"
        "s_waitcnt lgkmcnt(9)\n"
        "v_mfma_f32_32x32x16_bf16 v[16:31], v[40:43], v[56:59], v[16:31]\n"
        "ds_read_b64_tr_b16 v[40:41], v3 offset:8192\n"
        "ds_read_b64_tr_b16 v[42:43], v3 offset:8704\n"
        "ds_read_b128 v[56:59], v6\n"
        "s_waitcnt lgkmcnt(9)\n"
        "v_mfma_f32_32x32x16_bf16 v[16:31], v[44:47], v[60:63], v[16:31]\n"
        "ds_read_b64_tr_b16 v[44:45], v3 offset:12288\n"
        "ds_read_b64_tr_b16 v[46:47], v3 offset:12800\n"
        "ds_read_b128 v[60:63], v7\n"
        "ds_read_b128 v[80:83], v8\n"
        "ds_read_b128 v[84:87], v9\n"
        "ds_read_b128 v[88:91], v10\n"
        "s_waitcnt lgkmcnt(12)\n"
        "v_mfma_f32_32x32x16_bf16 v[16:31], v[32:35], v[48:51], v[16:31]\n"
        "ds_read_b128 v[48:51], v11\n"
        "s_waitcnt lgkmcnt(10)\n"
        "v_mfma_f32_32x32x16_bf16 v[16:31], v[36:39], v[52:55], v[16:31]\n"
        "s_barrier\n"
        "ds_read_b64_tr_b16 v[64:65], v3 offset:16384\n"
        "ds_read_b64_tr_b16 v[66:67], v3 offset:16896\n"
        "ds_read_b64_tr_b16 v[68:69], v3 offset:20480\n"
        "ds_read_b64_tr_b16 v[70:71], v3 offset:20992\n"
        "s_waitcnt lgkmcnt(11)\n"
        "v_mfma_f32_32x32x16_bf16 v[16:31], v[40:43], v[56:59], v[16:31]\n"
        "s_waitcnt lgkmcnt(8)\n"
        "v_mfma_f32_32x32x16_bf16 v[16:31], v[44:47], v[60:63], v[16:31]\n"
        "s_barrier\n"
        "ds_read_b64_tr_b16 v[72:73], v3 offset:24576\n"
        "ds_read_b64_tr_b16 v[74:75], v3 offset:25088\n"
        "ds_read_b64_tr_b16 v[76:77], v3 offset:28672\n"
        "ds_read_b64_tr_b16 v[78:79], v3 offset:29184\n"
        "s_waitcnt lgkmcnt(6)\n"
        "v_mfma_f32_32x32x16_bf16 v[16:31], v[64:67], v[80:83], v[16:31]\n"
        "s_waitcnt lgkmcnt(4)\n"
        "v_mfma_f32_32x32x16_bf16 v[16:31], v[68:71], v[84:87], v[16:31]\n"
        "v_mov_b32 v4, v250\n"
        "v_xor_b32 v5, 32, v250\n"
        "v_xor_b32 v6, 64, v250\n"
        "v_xor_b32 v7, 0x60, v250\n"
        "s_waitcnt lgkmcnt(2)\n"
        "v_mfma_f32_32x32x16_bf16 v[16:31], v[72:75], v[88:91], v[16:31]\n"
        "s_waitcnt lgkmcnt(0)\n"
        "v_mfma_f32_32x32x16_bf16 v[16:31], v[76:79], v[48:51], v[16:31]\n"
        "s_cmpk_ge_u32 %[b], 0xf0\n"
        "s_nop 10\n"
        "v_pk_mul_f32 v[32:33], v[16:17], v[16:17]\n"
        "v_pk_mul_f32 v[34:35], v[18:19], v[18:19]\n"
        "v_pk_mul_f32 v[36:37], v[20:21], v[20:21]\n"
        "v_pk_mul_f32 v[38:39], v[22:23], v[22:23]\n"
        "v_pk_fma_f32 v[32:33], v[32:33], s[26:27], v[94:95]\n"
        "v_pk_fma_f32 v[34:35], v[34:35], s[26:27], v[94:95]\n"
        "v_pk_fma_f32 v[36:37], v[36:37], s[26:27], v[94:95]\n"
        "v_pk_fma_f32 v[38:39], v[38:39], s[26:27], v[94:95]\n"
        "v_pk_mul_f32 v[32:33], v[32:33], v[16:17]\n"
        "v_pk_mul_f32 v[34:35], v[34:35], v[18:19]\n"
        "v_pk_mul_f32 v[36:37], v[36:37], v[20:21]\n"
        "v_pk_mul_f32 v[38:39], v[38:39], v[22:23]\n"
        "v_exp_f32 v32, v32\n"
        "v_exp_f32 v33, v33\n"
        "v_exp_f32 v34, v34\n"
        "v_exp_f32 v35, v35\n"
        "v_exp_f32 v36, v36\n"
        "v_exp_f32 v37, v37\n"
        "v_exp_f32 v38, v38\n"
        "v_exp_f32 v39, v39\n"
        "v_pk_add_f32 v[32:33], v[32:33], 1.0 op_sel_hi:[1,0]\n"
        "v_pk_add_f32 v[34:35], v[34:35], 1.0 op_sel_hi:[1,0]\n"
        "v_pk_add_f32 v[36:37], v[36:37], 1.0 op_sel_hi:[1,0]\n"
        "v_pk_add_f32 v[38:39], v[38:39], 1.0 op_sel_hi:[1,0]\n"
        "v_rcp_f32 v32, v32\n"
        "v_rcp_f32 v33, v33\n"
        "v_rcp_f32 v34, v34\n"
        "v_rcp_f32 v35, v35\n"
        "v_rcp_f32 v36, v36\n"
        "v_rcp_f32 v37, v37\n"
        "v_rcp_f32 v38, v38\n"
        "v_rcp_f32 v39, v39\n"
        "v_pk_mul_f32 v[32:33], v[32:33], v[16:17]\n"
        "v_pk_mul_f32 v[34:35], v[34:35], v[18:19]\n"
        "v_pk_mul_f32 v[36:37], v[36:37], v[20:21]\n"
        "v_pk_mul_f32 v[38:39], v[38:39], v[22:23]\n"
        "v_pk_mul_f32 v[32:33], v[32:33], v[24:25]\n"
        "v_pk_mul_f32 v[34:35], v[34:35], v[26:27]\n"
        "v_pk_mul_f32 v[36:37], v[36:37], v[28:29]\n"
        "v_pk_mul_f32 v[38:39], v[38:39], v[30:31]\n"
        "v_cvt_pk_bf16_f32 v8, v32, v33\n"
        "v_cvt_pk_bf16_f32 v9, v34, v35\n"
        "v_cvt_pk_bf16_f32 v10, v36, v37\n"
        "v_cvt_pk_bf16_f32 v11, v38, v39\n"
        "s_cbranch_scc1 L_nost%=\n"
        "buffer_store_dwordx4 v[8:11], v12, s[20:23], 0 offen sc1\n"
        "v_add3_u32 v14, v8, v9, v10\n"
        "v_sub_u32 v15, s12, v11\n"
        "v_sub_u32 v14, v15, v14\n"
        "buffer_store_dword v14, v13, s[20:23], 0 offen offset:1024 sc1\n"
        "L_nost%=:\n"
        "s_barrier\n"
        "ds_read_b64_tr_b16 v[104:105], v4 offset:0\n"
        "ds_read_b64_tr_b16 v[106:107], v4 offset:512\n"
        "ds_read_b64_tr_b16 v[108:109], v5 offset:0\n"
        "ds_read_b64_tr_b16 v[110:111], v5 offset:512\n"
        "ds_read_b64_tr_b16 v[112:113], v6 offset:0\n"
        "ds_read_b64_tr_b16 v[114:115], v6 offset:512\n"
        "ds_read_b64_tr_b16 v[116:117], v7 offset:0\n"
        "ds_read_b64_tr_b16 v[118:119], v7 offset:512\n"
        "ds_read_b64_tr_b16 v[120:121], v4 offset:4096\n"
        "ds_read_b64_tr_b16 v[122:123], v4 offset:4608\n"
        "ds_read_b64_tr_b16 v[124:125], v5 offset:4096\n"
        "ds_read_b64_tr_b16 v[126:127], v5 offset:4608\n"
        "ds_read_b64_tr_b16 v[128:129], v6 offset:4096\n"
        "ds_read_b64_tr_b16 v[130:131], v6 offset:4608\n"
        "ds_read_b64_tr_b16 v[132:133], v7 offset:4096\n"
        "ds_read_b64_tr_b16 v[134:135], v7 offset:4608\n"
        "ds_read_b64_tr_b16 v[136:137], v4 offset:8192\n"
        "ds_read_b64_tr_b16 v[138:139], v4 offset:8704\n"
        "ds_read_b64_tr_b16 v[140:141], v5 offset:8192\n"
        "ds_read_b64_tr_b16 v[142:143], v5 offset:8704\n"
        "ds_read_b64_tr_b16 v[144:145], v6 offset:8192\n"
        "ds_read_b64_tr_b16 v[146:147], v6 offset:8704\n"
        "ds_read_b64_tr_b16 v[148:149], v7 offset:8192\n"
        "ds_read_b64_tr_b16 v[150:151], v7 offset:8704\n"
        "ds_read_b64_tr_b16 v[152:153], v4 offset:12288\n"
        "ds_read_b64_tr_b16 v[154:155], v4 offset:12800\n"
        "ds_read_b64_tr_b16 v[156:157], v5 offset:12288\n"
        "ds_read_b64_tr_b16 v[158:159], v5 offset:12800\n"
        "ds_read_b64_tr_b16 v[160:161], v6 offset:12288\n"
        "ds_read_b64_tr_b16 v[162:163], v6 offset:12800\n"
        "ds_read_b64_tr_b16 v[164:165], v7 offset:12288\n"
        "ds_read_b64_tr_b16 v[166:167], v7 offset:12800\n"
        "ds_read_b64_tr_b16 v[168:169], v4 offset:16384\n"
        "ds_read_b64_tr_b16 v[170:171], v4 offset:16896\n"
        "ds_read_b64_tr_b16 v[172:173], v5 offset:16384\n"
        "ds_read_b64_tr_b16 v[174:175], v5 offset:16896\n"
        "ds_read_b64_tr_b16 v[176:177], v6 offset:16384\n"
        "ds_read_b64_tr_b16 v[178:179], v6 offset:16896\n"
        "ds_read_b64_tr_b16 v[180:181], v7 offset:16384\n"
        "ds_read_b64_tr_b16 v[182:183], v7 offset:16896\n"
        "ds_read_b64_tr_b16 v[184:185], v4 offset:20480\n"
        "ds_read_b64_tr_b16 v[186:187], v4 offset:20992\n"
        "ds_read_b64_tr_b16 v[188:189], v5 offset:20480\n"
        "ds_read_b64_tr_b16 v[190:191], v5 offset:20992\n"
        "ds_read_b64_tr_b16 v[192:193], v6 offset:20480\n"
        "ds_read_b64_tr_b16 v[194:195], v6 offset:20992\n"
        "ds_read_b64_tr_b16 v[196:197], v7 offset:20480\n"
        "ds_read_b64_tr_b16 v[198:199], v7 offset:20992\n"
        "ds_read_b64_tr_b16 v[200:201], v4 offset:24576\n"
        "ds_read_b64_tr_b16 v[202:203], v4 offset:25088\n"
        "ds_read_b64_tr_b16 v[204:205], v5 offset:24576\n"
        "ds_read_b64_tr_b16 v[206:207], v5 offset:25088\n"
        "ds_read_b64_tr_b16 v[208:209], v6 offset:24576\n"
        "ds_read_b64_tr_b16 v[210:211], v6 offset:25088\n"
        "ds_read_b64_tr_b16 v[212:213], v7 offset:24576\n"
        "ds_read_b64_tr_b16 v[214:215], v7 offset:25088\n"
        "ds_read_b64_tr_b16 v[216:217], v4 offset:28672\n"
        "ds_read_b64_tr_b16 v[218:219], v4 offset:29184\n"
        "ds_read_b64_tr_b16 v[220:221], v5 offset:28672\n"
        "ds_read_b64_tr_b16 v[222:223], v5 offset:29184\n"
        "ds_read_b64_tr_b16 v[224:225], v6 offset:28672\n"
        "ds_read_b64_tr_b16 v[226:227], v6 offset:29184\n"
        "ds_read_b64_tr_b16 v[228:229], v7 offset:28672\n"
        "ds_read_b64_tr_b16 v[230:231], v7 offset:29184\n"
        "s_barrier\n"
        "buffer_load_dwordx4 v[32:35], v248, s[20:23], 0 offen sc1\n"
        "buffer_load_dword v88, v249, s[20:23], 0 offen sc1\n"
        "buffer_load_dwordx4 v[36:39], v248, s[20:23], 0 offen offset:2560 sc1\n"
        "buffer_load_dword v89, v249, s[20:23], 0 offen offset:2560 sc1\n"
        "buffer_load_dwordx4 v[40:43], v248, s[20:23], s33 offen sc1\n"
        "buffer_load_dword v90, v249, s[20:23], s33 offen sc1\n"
        "buffer_load_dwordx4 v[44:47], v248, s[20:23], s33 offen offset:2560 sc1\n"
        "buffer_load_dword v91, v249, s[20:23], s33 offen offset:2560 sc1\n"
        "buffer_load_dwordx4 v[48:51], v248, s[20:23], s34 offen sc1\n"
        "buffer_load_dword v92, v249, s[20:23], s34 offen sc1\n"
        "buffer_load_dwordx4 v[52:55], v248, s[20:23], s34 offen offset:2560 sc1\n"
        "buffer_load_dword v93, v249, s[20:23], s34 offen offset:2560 sc1\n"
        "buffer_load_dwordx4 v[56:59], v248, s[20:23], s35 offen sc1\n"
        "buffer_load_dword v94, v249, s[20:23], s35 offen sc1\n"
        "s_barrier\n"
        "ds_read_b64_tr_b16 v[232:233], v4 offset:32768\n"
        "ds_read_b64_tr_b16 v[234:235], v4 offset:33280\n"
        "ds_read_b64_tr_b16 v[236:237], v5 offset:32768\n"
        "ds_read_b64_tr_b16 v[238:239], v5 offset:33280\n"
        "ds_read_b64_tr_b16 v[240:241], v6 offset:32768\n"
        "ds_read_b64_tr_b16 v[242:243], v6 offset:33280\n"
        "ds_read_b64_tr_b16 v[244:245], v7 offset:32768\n"
        "ds_read_b64_tr_b16 v[246:247], v7 offset:33280\n"
        "buffer_load_dwordx4 v[60:63], v248, s[20:23], s35 offen offset:2560 sc1\n"
        "buffer_load_dword v95, v249, s[20:23], s35 offen offset:2560 sc1\n"
        "s_waitcnt vmcnt(14) lgkmcnt(8)\n"
        "v_mfma_f32_16x16x32_bf16 v[16:19], v[104:107], v[32:35], 0\n"
        "ds_read_b64_tr_b16 v[104:105], v4 offset:36864\n"
        "ds_read_b64_tr_b16 v[106:107], v4 offset:37376\n"
        "v_add3_u32 v13, v32, v33, v34\n"
        "v_mfma_f32_16x16x32_bf16 v[20:23], v[108:111], v[32:35], 0\n"
        "ds_read_b64_tr_b16 v[108:109], v5 offset:36864\n"
        "ds_read_b64_tr_b16 v[110:111], v5 offset:37376\n"
        "v_add3_u32 v13, v13, v35, v88\n"
        "v_mfma_f32_16x16x32_bf16 v[24:27], v[112:115], v[32:35], 0\n"
        "ds_read_b64_tr_b16 v[112:113], v6 offset:36864\n"
        "ds_read_b64_tr_b16 v[114:115], v6 offset:37376\n"
        "v_xor_b32 v12, s12, v13\n"
        "v_mfma_f32_16x16x32_bf16 v[28:31], v[116:119], v[32:35], 0\n"
        "ds_read_b64_tr_b16 v[116:117], v7 offset:36864\n"
        "ds_read_b64_tr_b16 v[118:119], v7 offset:37376\n"
        "buffer_load_dwordx4 v[64:67], v248, s[20:23], s36 offen sc1\n"
        "buffer_load_dword v96, v249, s[20:23], s36 offen sc1\n"
        "s_waitcnt vmcnt(14)\n"
        "v_mfma_f32_16x16x32_bf16 v[16:19], v[120:123], v[36:39], v[16:19]\n"
        "ds_read_b64_tr_b16 v[120:121], v4 offset:40960\n"
        "ds_read_b64_tr_b16 v[122:123], v4 offset:41472\n"
        "v_add3_u32 v13, v36, v37, v38\n"
        "v_mfma_f32_16x16x32_bf16 v[20:23], v[124:127], v[36:39], v[20:23]\n"
        "ds_read_b64_tr_b16 v[124:125], v5 offset:40960\n"
        "ds_read_b64_tr_b16 v[126:127], v5 offset:41472\n"
        "v_add3_u32 v13, v13, v39, v89\n"
        "v_mfma_f32_16x16x32_bf16 v[24:27], v[128:131], v[36:39], v[24:27]\n"
        "ds_read_b64_tr_b16 v[128:129], v6 offset:40960\n"
        "ds_read_b64_tr_b16 v[130:131], v6 offset:41472\n"
        "v_bitop3_b32 v12, v13, v12, s12 bitop3:0xde\n"
        "v_mfma_f32_16x16x32_bf16 v[28:31], v[132:135], v[36:39], v[28:31]\n"
        "ds_read_b64_tr_b16 v[132:133], v7 offset:40960\n"
        "ds_read_b64_tr_b16 v[134:135], v7 offset:41472\n"
        "buffer_load_dwordx4 v[68:71], v248, s[20:23], s36 offen offset:2560 sc1\n"
        "buffer_load_dword v97, v249, s[20:23], s36 offen offset:2560 sc1\n"
        "s_waitcnt vmcnt(14)\n"
        "v_mfma_f32_16x16x32_bf16 v[16:19], v[136:139], v[40:43], v[16:19]\n"
        "ds_read_b64_tr_b16 v[136:137], v4 offset:45056\n"
        "ds_read_b64_tr_b16 v[138:139], v4 offset:45568\n"
        "v_add3_u32 v13, v40, v41, v42\n"
        "v_mfma_f32_16x16x32_bf16 v[20:23], v[140:143], v[40:43], v[20:23]\n"
        "ds_read_b64_tr_b16 v[140:141], v5 offset:45056\n"
        "ds_read_b64_tr_b16 v[142:143], v5 offset:45568\n"
        "v_add3_u32 v13, v13, v43, v90\n"
        "v_mfma_f32_16x16x32_bf16 v[24:27], v[144:147], v[40:43], v[24:27]\n"
        "ds_read_b64_tr_b16 v[144:145], v6 offset:45056\n"
        "ds_read_b64_tr_b16 v[146:147], v6 offset:45568\n"
        "v_bitop3_b32 v12, v13, v12, s12 bitop3:0xde\n"
        "v_mfma_f32_16x16x32_bf16 v[28:31], v[148:151], v[40:43], v[28:31]\n"
        "ds_read_b64_tr_b16 v[148:149], v7 offset:45056\n"
        "ds_read_b64_tr_b16 v[150:151], v7 offset:45568\n"
        "buffer_load_dwordx4 v[72:75], v248, s[20:23], s37 offen sc1\n"
        "buffer_load_dword v98, v249, s[20:23], s37 offen sc1\n"
        "s_waitcnt vmcnt(14)\n"
        "v_mfma_f32_16x16x32_bf16 v[16:19], v[152:155], v[44:47], v[16:19]\n"
        "ds_read_b64_tr_b16 v[152:153], v4 offset:49152\n"
        "ds_read_b64_tr_b16 v[154:155], v4 offset:49664\n"
        "v_add3_u32 v13, v44, v45, v46\n"
        "v_mfma_f32_16x16x32_bf16 v[20:23], v[156:159], v[44:47], v[20:23]\n"
        "ds_read_b64_tr_b16 v[156:157], v5 offset:49152\n"
        "ds_read_b64_tr_b16 v[158:159], v5 offset:49664\n"
        "v_add3_u32 v13, v13, v47, v91\n"
        "v_mfma_f32_16x16x32_bf16 v[24:27], v[160:163], v[44:47], v[24:27]\n"
        "ds_read_b64_tr_b16 v[160:161], v6 offset:49152\n"
        "ds_read_b64_tr_b16 v[162:163], v6 offset:49664\n"
        "v_bitop3_b32 v12, v13, v12, s12 bitop3:0xde\n"
        "v_mfma_f32_16x16x32_bf16 v[28:31], v[164:167], v[44:47], v[28:31]\n"
        "ds_read_b64_tr_b16 v[164:165], v7 offset:49152\n"
        "ds_read_b64_tr_b16 v[166:167], v7 offset:49664\n"
        "buffer_load_dwordx4 v[76:79], v248, s[20:23], s37 offen offset:2560 sc1\n"
        "buffer_load_dword v99, v249, s[20:23], s37 offen offset:2560 sc1\n"
        "s_waitcnt vmcnt(14)\n"
        "v_mfma_f32_16x16x32_bf16 v[16:19], v[168:171], v[48:51], v[16:19]\n"
        "ds_read_b64_tr_b16 v[168:169], v4 offset:53248\n"
        "ds_read_b64_tr_b16 v[170:171], v4 offset:53760\n"
        "v_add3_u32 v13, v48, v49, v50\n"
        "v_mfma_f32_16x16x32_bf16 v[20:23], v[172:175], v[48:51], v[20:23]\n"
        "ds_read_b64_tr_b16 v[172:173], v5 offset:53248\n"
        "ds_read_b64_tr_b16 v[174:175], v5 offset:53760\n"
        "v_add3_u32 v13, v13, v51, v92\n"
        "v_mfma_f32_16x16x32_bf16 v[24:27], v[176:179], v[48:51], v[24:27]\n"
        "ds_read_b64_tr_b16 v[176:177], v6 offset:53248\n"
        "ds_read_b64_tr_b16 v[178:179], v6 offset:53760\n"
        "v_bitop3_b32 v12, v13, v12, s12 bitop3:0xde\n"
        "v_mfma_f32_16x16x32_bf16 v[28:31], v[180:183], v[48:51], v[28:31]\n"
        "ds_read_b64_tr_b16 v[180:181], v7 offset:53248\n"
        "ds_read_b64_tr_b16 v[182:183], v7 offset:53760\n"
        "buffer_load_dwordx4 v[80:83], v248, s[20:23], s38 offen sc1\n"
        "buffer_load_dword v100, v249, s[20:23], s38 offen sc1\n"
        "s_waitcnt vmcnt(14) lgkmcnt(8)\n"
        "v_mfma_f32_16x16x32_bf16 v[16:19], v[184:187], v[52:55], v[16:19]\n"
        "v_add3_u32 v13, v52, v53, v54\n"
        "v_mfma_f32_16x16x32_bf16 v[20:23], v[188:191], v[52:55], v[20:23]\n"
        "v_add3_u32 v13, v13, v55, v93\n"
        "v_mfma_f32_16x16x32_bf16 v[24:27], v[192:195], v[52:55], v[24:27]\n"
        "v_bitop3_b32 v12, v13, v12, s12 bitop3:0xde\n"
        "v_mfma_f32_16x16x32_bf16 v[28:31], v[196:199], v[52:55], v[28:31]\n"
        "buffer_load_dwordx4 v[84:87], v248, s[20:23], s38 offen offset:2560 sc1\n"
        "buffer_load_dword v101, v249, s[20:23], s38 offen offset:2560 sc1\n"
        "s_waitcnt vmcnt(14)\n"
        "v_mfma_f32_16x16x32_bf16 v[16:19], v[200:203], v[56:59], v[16:19]\n"
        "v_add3_u32 v13, v56, v57, v58\n"
        "v_mfma_f32_16x16x32_bf16 v[20:23], v[204:207], v[56:59], v[20:23]\n"
        "v_add3_u32 v13, v13, v59, v94\n"
        "v_mfma_f32_16x16x32_bf16 v[24:27], v[208:211], v[56:59], v[24:27]\n"
        "v_bitop3_b32 v12, v13, v12, s12 bitop3:0xde\n"
        "v_mfma_f32_16x16x32_bf16 v[28:31], v[212:215], v[56:59], v[28:31]\n"
        "s_waitcnt vmcnt(12)\n"
        "v_mfma_f32_16x16x32_bf16 v[16:19], v[216:219], v[60:63], v[16:19]\n"
        "v_add3_u32 v13, v60, v61, v62\n"
        "v_mfma_f32_16x16x32_bf16 v[20:23], v[220:223], v[60:63], v[20:23]\n"
        "v_add3_u32 v13, v13, v63, v95\n"
        "v_mfma_f32_16x16x32_bf16 v[24:27], v[224:227], v[60:63], v[24:27]\n"
        "v_bitop3_b32 v12, v13, v12, s12 bitop3:0xde\n"
        "v_mfma_f32_16x16x32_bf16 v[28:31], v[228:231], v[60:63], v[28:31]\n"
        "s_waitcnt vmcnt(10)\n"
        "v_mfma_f32_16x16x32_bf16 v[16:19], v[232:235], v[64:67], v[16:19]\n"
        "v_add3_u32 v13, v64, v65, v66\n"
        "v_mfma_f32_16x16x32_bf16 v[20:23], v[236:239], v[64:67], v[20:23]\n"
        "v_add3_u32 v13, v13, v67, v96\n"
        "v_mfma_f32_16x16x32_bf16 v[24:27], v[240:243], v[64:67], v[24:27]\n"
        "v_bitop3_b32 v12, v13, v12, s12 bitop3:0xde\n"
        "v_mfma_f32_16x16x32_bf16 v[28:31], v[244:247], v[64:67], v[28:31]\n"
        "s_waitcnt vmcnt(8) lgkmcnt(0)\n"
        "v_mfma_f32_16x16x32_bf16 v[16:19], v[104:107], v[68:71], v[16:19]\n"
        "v_add3_u32 v13, v68, v69, v70\n"
        "v_mfma_f32_16x16x32_bf16 v[20:23], v[108:111], v[68:71], v[20:23]\n"
        "v_add3_u32 v13, v13, v71, v97\n"
        "v_mfma_f32_16x16x32_bf16 v[24:27], v[112:115], v[68:71], v[24:27]\n"
        "v_bitop3_b32 v12, v13, v12, s12 bitop3:0xde\n"
        "v_mfma_f32_16x16x32_bf16 v[28:31], v[116:119], v[68:71], v[28:31]\n"
        "s_waitcnt vmcnt(6)\n"
        "v_mfma_f32_16x16x32_bf16 v[16:19], v[120:123], v[72:75], v[16:19]\n"
        "v_add3_u32 v13, v72, v73, v74\n"
        "v_mfma_f32_16x16x32_bf16 v[20:23], v[124:127], v[72:75], v[20:23]\n"
        "v_add3_u32 v13, v13, v75, v98\n"
        "v_mfma_f32_16x16x32_bf16 v[24:27], v[128:131], v[72:75], v[24:27]\n"
        "v_bitop3_b32 v12, v13, v12, s12 bitop3:0xde\n"
        "v_mfma_f32_16x16x32_bf16 v[28:31], v[132:135], v[72:75], v[28:31]\n"
        "s_waitcnt vmcnt(4)\n"
        "v_mfma_f32_16x16x32_bf16 v[16:19], v[136:139], v[76:79], v[16:19]\n"
        "v_add3_u32 v13, v76, v77, v78\n"
        "v_mfma_f32_16x16x32_bf16 v[20:23], v[140:143], v[76:79], v[20:23]\n"
        "v_add3_u32 v13, v13, v79, v99\n"
        "v_mfma_f32_16x16x32_bf16 v[24:27], v[144:147], v[76:79], v[24:27]\n"
        "v_bitop3_b32 v12, v13, v12, s12 bitop3:0xde\n"
        "v_mfma_f32_16x16x32_bf16 v[28:31], v[148:151], v[76:79], v[28:31]\n"
        "s_waitcnt vmcnt(2)\n"
        "v_mfma_f32_16x16x32_bf16 v[16:19], v[152:155], v[80:83], v[16:19]\n"
        "v_add3_u32 v13, v80, v81, v82\n"
        "v_mfma_f32_16x16x32_bf16 v[20:23], v[156:159], v[80:83], v[20:23]\n"
        "v_add3_u32 v13, v13, v83, v100\n"
        "v_mfma_f32_16x16x32_bf16 v[24:27], v[160:163], v[80:83], v[24:27]\n"
        "v_bitop3_b32 v12, v13, v12, s12 bitop3:0xde\n"
        "v_mfma_f32_16x16x32_bf16 v[28:31], v[164:167], v[80:83], v[28:31]\n"
        "s_waitcnt vmcnt(0)\n"
        "v_mfma_f32_16x16x32_bf16 v[16:19], v[168:171], v[84:87], v[16:19]\n"
        "v_add3_u32 v13, v84, v85, v86\n"
        "v_mfma_f32_16x16x32_bf16 v[20:23], v[172:175], v[84:87], v[20:23]\n"
        "v_add3_u32 v13, v13, v87, v101\n"
        "v_mfma_f32_16x16x32_bf16 v[24:27], v[176:179], v[84:87], v[24:27]\n"
        "v_bitop3_b32 v12, v13, v12, s12 bitop3:0xde\n"
        "v_mfma_f32_16x16x32_bf16 v[28:31], v[180:183], v[84:87], v[28:31]\n"
        "v_cmp_ne_u32 vcc, 0, v12\n"
        "s_cbranch_vccnz L_fix%=\n"
        "L_st%=:\n"
        "s_nop 2\n"
        "v_perm_b32 v16, v16, v255, s41\n"
        "v_perm_b32 v17, v17, v255, s42\n"
        "v_perm_b32 v18, v18, v255, s43\n"
        "v_perm_b32 v19, v19, v255, s46\n"
        "buffer_store_dwordx4 v[16:19], v251, s[20:23], 0 offen offset:0 sc1\n"
        "v_perm_b32 v20, v20, v255, s41\n"
        "v_perm_b32 v21, v21, v255, s42\n"
        "v_perm_b32 v22, v22, v255, s43\n"
        "v_perm_b32 v23, v23, v255, s46\n"
        "buffer_store_dwordx4 v[20:23], v251, s[20:23], 0 offen offset:512 sc1\n"
        "v_perm_b32 v24, v24, v255, s41\n"
        "v_perm_b32 v25, v25, v255, s42\n"
        "v_perm_b32 v26, v26, v255, s43\n"
        "v_perm_b32 v27, v27, v255, s46\n"
        "buffer_store_dwordx4 v[24:27], v251, s[20:23], 0 offen offset:1024 sc1\n"
        "v_perm_b32 v28, v28, v255, s41\n"
        "v_perm_b32 v29, v29, v255, s42\n"
        "v_perm_b32 v30, v30, v255, s43\n"
        "v_perm_b32 v31, v31, v255, s46\n"
        "buffer_store_dwordx4 v[28:31], v251, s[20:23], 0 offen offset:1536 sc1\n"
        "s_sleep 16\n"
        "L_red%=:\n"
        "buffer_load_dwordx4 v[32:35], v252, s[20:23], 0 offen sc1\n"
        "buffer_load_dwordx4 v[36:39], v252, s[20:23], s52 offen sc1\n"
        "buffer_load_dwordx4 v[40:43], v252, s[20:23], s53 offen sc1\n"
        "buffer_load_dwordx4 v[44:47], v252, s[20:23], s54 offen sc1\n"
        "s_waitcnt vmcnt(3)\n"
        "v_xor_b32 v32, s48, v32\n"
        "v_xor_b32 v33, s49, v33\n"
        "v_xor_b32 v34, s50, v34\n"
        "v_xor_b32 v35, s51, v35\n"
        "v_or3_b32 v12, v32, v33, v34\n"
        "v_or_b32 v12, v12, v35\n"
        "s_waitcnt vmcnt(2)\n"
        "v_xor_b32 v36, s48, v36\n"
        "v_xor_b32 v37, s49, v37\n"
        "v_xor_b32 v38, s50, v38\n"
        "v_xor_b32 v39, s51, v39\n"
        "v_or3_b32 v12, v12, v36, v37\n"
        "v_or3_b32 v12, v12, v38, v39\n"
        "v_pk_add_f32 v[32:33], v[32:33], v[36:37]\n"
        "v_pk_add_f32 v[34:35], v[34:35], v[38:39]\n"
        "s_waitcnt vmcnt(1)\n"
        "v_xor_b32 v40, s48, v40\n"
        "v_xor_b32 v41, s49, v41\n"
        "v_xor_b32 v42, s50, v42\n"
        "v_xor_b32 v43, s51, v43\n"
        "v_or3_b32 v12, v12, v40, v41\n"
        "v_or3_b32 v12, v12, v42, v43\n"
        "v_pk_add_f32 v[32:33], v[32:33], v[40:41]\n"
        "v_pk_add_f32 v[34:35], v[34:35], v[42:43]\n"
        "s_waitcnt vmcnt(0)\n"
        "v_xor_b32 v44, s48, v44\n"
        "v_xor_b32 v45, s49, v45\n"
        "v_xor_b32 v46, s50, v46\n"
        "v_xor_b32 v47, s51, v47\n"
        "v_or3_b32 v12, v12, v44, v45\n"
        "v_or3_b32 v12, v12, v46, v47\n"
        "v_pk_add_f32 v[32:33], v[32:33], v[44:45]\n"
        "v_pk_add_f32 v[34:35], v[34:35], v[46:47]\n"
        "v_and_b32 v12, 0xff, v12\n"
        "v_cmp_ne_u32 vcc, 0, v12\n"
        "v_permlane32_swap_b32 v32, v34\n"
        "v_permlane32_swap_b32 v33, v35\n"
        "s_cbranch_vccnz L_red2%=\n"
        "L_fin%=:\n"
        "v_pk_add_f32 v[32:33], v[32:33], v[34:35]\n"
        "s_add_u32 s24, %[b], s17\n"
        "v_cvt_pk_bf16_f32 v32, v32, v33\n"
        "global_store_dword v253, v32, s[8:9]\n"
        "s_cmpk_lg_u32 s24, 0x80\n"
        "s_cbranch_scc1 L_noadv%=\n"
        "s_mov_b64 exec, 1\n"
        "buffer_store_dword v255, off, s[20:23], 0 sc1\n"
        "L_noadv%=:\n"
        "s_endpgm\n"
        "L_red2%=:\n"
        "s_sub_u32 s40, s40, 1\n"
        "s_cbranch_scc0 L_red%=\n"
        "s_branch L_fin%=\n"
        "L_fix%=:\n"
        "s_mov_b32 s47, 0\n"
        "v_add3_u32 v13, v32, v33, v34\n"
        "v_add3_u32 v13, v13, v35, v88\n"
        "v_cmp_ne_u32 vcc, s12, v13\n"
        "s_cbranch_vccz L_ok0%=\n"
        "buffer_load_dwordx4 v[32:35], v248, s[20:23], 0 offen sc1\n"
        "buffer_load_dword v88, v249, s[20:23], 0 offen sc1\n"
        "s_mov_b32 s47, 1\n"
        "L_ok0%=:\n"
        "v_add3_u32 v13, v36, v37, v38\n"
        "v_add3_u32 v13, v13, v39, v89\n"
        "v_cmp_ne_u32 vcc, s12, v13\n"
        "s_cbranch_vccz L_ok1%=\n"
        "buffer_load_dwordx4 v[36:39], v248, s[20:23], 0 offen offset:2560 sc1\n"
        "buffer_load_dword v89, v249, s[20:23], 0 offen offset:2560 sc1\n"
        "s_mov_b32 s47, 1\n"
        "L_ok1%=:\n"
        "v_add3_u32 v13, v40, v41, v42\n"
        "v_add3_u32 v13, v13, v43, v90\n"
        "v_cmp_ne_u32 vcc, s12, v13\n"
        "s_cbranch_vccz L_ok2%=\n"
        "buffer_load_dwordx4 v[40:43], v248, s[20:23], s33 offen sc1\n"
        "buffer_load_dword v90, v249, s[20:23], s33 offen sc1\n"
        "s_mov_b32 s47, 1\n"
        "L_ok2%=:\n"
        "v_add3_u32 v13, v44, v45, v46\n"
        "v_add3_u32 v13, v13, v47, v91\n"
        "v_cmp_ne_u32 vcc, s12, v13\n"
        "s_cbranch_vccz L_ok3%=\n"
        "buffer_load_dwordx4 v[44:47], v248, s[20:23], s33 offen offset:2560 sc1\n"
        "buffer_load_dword v91, v249, s[20:23], s33 offen offset:2560 sc1\n"
        "s_mov_b32 s47, 1\n"
        "L_ok3%=:\n"
        "v_add3_u32 v13, v48, v49, v50\n"
        "v_add3_u32 v13, v13, v51, v92\n"
        "v_cmp_ne_u32 vcc, s12, v13\n"
        "s_cbranch_vccz L_ok4%=\n"
        "buffer_load_dwordx4 v[48:51], v248, s[20:23], s34 offen sc1\n"
        "buffer_load_dword v92, v249, s[20:23], s34 offen sc1\n"
        "s_mov_b32 s47, 1\n"
        "L_ok4%=:\n"
        "v_add3_u32 v13, v52, v53, v54\n"
        "v_add3_u32 v13, v13, v55, v93\n"
        "v_cmp_ne_u32 vcc, s12, v13\n"
        "s_cbranch_vccz L_ok5%=\n"
        "buffer_load_dwordx4 v[52:55], v248, s[20:23], s34 offen offset:2560 sc1\n"
        "buffer_load_dword v93, v249, s[20:23], s34 offen offset:2560 sc1\n"
        "s_mov_b32 s47, 1\n"
        "L_ok5%=:\n"
        "v_add3_u32 v13, v56, v57, v58\n"
        "v_add3_u32 v13, v13, v59, v94\n"
        "v_cmp_ne_u32 vcc, s12, v13\n"
        "s_cbranch_vccz L_ok6%=\n"
        "buffer_load_dwordx4 v[56:59], v248, s[20:23], s35 offen sc1\n"
        "buffer_load_dword v94, v249, s[20:23], s35 offen sc1\n"
        "s_mov_b32 s47, 1\n"
        "L_ok6%=:\n"
        "v_add3_u32 v13, v60, v61, v62\n"
        "v_add3_u32 v13, v13, v63, v95\n"
        "v_cmp_ne_u32 vcc, s12, v13\n"
        "s_cbranch_vccz L_ok7%=\n"
        "buffer_load_dwordx4 v[60:63], v248, s[20:23], s35 offen offset:2560 sc1\n"
        "buffer_load_dword v95, v249, s[20:23], s35 offen offset:2560 sc1\n"
        "s_mov_b32 s47, 1\n"
        "L_ok7%=:\n"
        "v_add3_u32 v13, v64, v65, v66\n"
        "v_add3_u32 v13, v13, v67, v96\n"
        "v_cmp_ne_u32 vcc, s12, v13\n"
        "s_cbranch_vccz L_ok8%=\n"
        "buffer_load_dwordx4 v[64:67], v248, s[20:23], s36 offen sc1\n"
        "buffer_load_dword v96, v249, s[20:23], s36 offen sc1\n"
        "s_mov_b32 s47, 1\n"
        "L_ok8%=:\n"
        "v_add3_u32 v13, v68, v69, v70\n"
        "v_add3_u32 v13, v13, v71, v97\n"
        "v_cmp_ne_u32 vcc, s12, v13\n"
        "s_cbranch_vccz L_ok9%=\n"
        "buffer_load_dwordx4 v[68:71], v248, s[20:23], s36 offen offset:2560 sc1\n"
        "buffer_load_dword v97, v249, s[20:23], s36 offen offset:2560 sc1\n"
        "s_mov_b32 s47, 1\n"
        "L_ok9%=:\n"
        "v_add3_u32 v13, v72, v73, v74\n"
        "v_add3_u32 v13, v13, v75, v98\n"
        "v_cmp_ne_u32 vcc, s12, v13\n"
        "s_cbranch_vccz L_ok10%=\n"
        "buffer_load_dwordx4 v[72:75], v248, s[20:23], s37 offen sc1\n"
        "buffer_load_dword v98, v249, s[20:23], s37 offen sc1\n"
        "s_mov_b32 s47, 1\n"
        "L_ok10%=:\n"
        "v_add3_u32 v13, v76, v77, v78\n"
        "v_add3_u32 v13, v13, v79, v99\n"
        "v_cmp_ne_u32 vcc, s12, v13\n"
        "s_cbranch_vccz L_ok11%=\n"
        "buffer_load_dwordx4 v[76:79], v248, s[20:23], s37 offen offset:2560 sc1\n"
        "buffer_load_dword v99, v249, s[20:23], s37 offen offset:2560 sc1\n"
        "s_mov_b32 s47, 1\n"
        "L_ok11%=:\n"
        "v_add3_u32 v13, v80, v81, v82\n"
        "v_add3_u32 v13, v13, v83, v100\n"
        "v_cmp_ne_u32 vcc, s12, v13\n"
        "s_cbranch_vccz L_ok12%=\n"
        "buffer_load_dwordx4 v[80:83], v248, s[20:23], s38 offen sc1\n"
        "buffer_load_dword v100, v249, s[20:23], s38 offen sc1\n"
        "s_mov_b32 s47, 1\n"
        "L_ok12%=:\n"
        "v_add3_u32 v13, v84, v85, v86\n"
        "v_add3_u32 v13, v13, v87, v101\n"
        "v_cmp_ne_u32 vcc, s12, v13\n"
        "s_cbranch_vccz L_ok13%=\n"
        "buffer_load_dwordx4 v[84:87], v248, s[20:23], s38 offen offset:2560 sc1\n"
        "buffer_load_dword v101, v249, s[20:23], s38 offen offset:2560 sc1\n"
        "s_mov_b32 s47, 1\n"
        "L_ok13%=:\n"
        "s_cmp_eq_u32 s47, 0\n"
        "s_cbranch_scc1 L_redo%=\n"
        "s_waitcnt vmcnt(0)\n"
        "s_sub_u32 s40, s40, 1\n"
        "s_cbranch_scc0 L_fix%=\n"
        "s_mov_b32 s40, 0\n"
        "L_redo%=:\n"
        "ds_read_b64_tr_b16 v[104:105], v4 offset:0\n"
        "ds_read_b64_tr_b16 v[106:107], v4 offset:512\n"
        "ds_read_b64_tr_b16 v[108:109], v5 offset:0\n"
        "ds_read_b64_tr_b16 v[110:111], v5 offset:512\n"
        "ds_read_b64_tr_b16 v[112:113], v6 offset:0\n"
        "ds_read_b64_tr_b16 v[114:115], v6 offset:512\n"
        "ds_read_b64_tr_b16 v[116:117], v7 offset:0\n"
        "ds_read_b64_tr_b16 v[118:119], v7 offset:512\n"
        "ds_read_b64_tr_b16 v[120:121], v4 offset:4096\n"
        "ds_read_b64_tr_b16 v[122:123], v4 offset:4608\n"
        "ds_read_b64_tr_b16 v[124:125], v5 offset:4096\n"
        "ds_read_b64_tr_b16 v[126:127], v5 offset:4608\n"
        "ds_read_b64_tr_b16 v[128:129], v6 offset:4096\n"
        "ds_read_b64_tr_b16 v[130:131], v6 offset:4608\n"
        "ds_read_b64_tr_b16 v[132:133], v7 offset:4096\n"
        "ds_read_b64_tr_b16 v[134:135], v7 offset:4608\n"
        "ds_read_b64_tr_b16 v[136:137], v4 offset:8192\n"
        "ds_read_b64_tr_b16 v[138:139], v4 offset:8704\n"
        "ds_read_b64_tr_b16 v[140:141], v5 offset:8192\n"
        "ds_read_b64_tr_b16 v[142:143], v5 offset:8704\n"
        "ds_read_b64_tr_b16 v[144:145], v6 offset:8192\n"
        "ds_read_b64_tr_b16 v[146:147], v6 offset:8704\n"
        "ds_read_b64_tr_b16 v[148:149], v7 offset:8192\n"
        "ds_read_b64_tr_b16 v[150:151], v7 offset:8704\n"
        "ds_read_b64_tr_b16 v[152:153], v4 offset:12288\n"
        "ds_read_b64_tr_b16 v[154:155], v4 offset:12800\n"
        "ds_read_b64_tr_b16 v[156:157], v5 offset:12288\n"
        "ds_read_b64_tr_b16 v[158:159], v5 offset:12800\n"
        "ds_read_b64_tr_b16 v[160:161], v6 offset:12288\n"
        "ds_read_b64_tr_b16 v[162:163], v6 offset:12800\n"
        "ds_read_b64_tr_b16 v[164:165], v7 offset:12288\n"
        "ds_read_b64_tr_b16 v[166:167], v7 offset:12800\n"
        "ds_read_b64_tr_b16 v[168:169], v4 offset:16384\n"
        "ds_read_b64_tr_b16 v[170:171], v4 offset:16896\n"
        "ds_read_b64_tr_b16 v[172:173], v5 offset:16384\n"
        "ds_read_b64_tr_b16 v[174:175], v5 offset:16896\n"
        "ds_read_b64_tr_b16 v[176:177], v6 offset:16384\n"
        "ds_read_b64_tr_b16 v[178:179], v6 offset:16896\n"
        "ds_read_b64_tr_b16 v[180:181], v7 offset:16384\n"
        "ds_read_b64_tr_b16 v[182:183], v7 offset:16896\n"
        "s_waitcnt lgkmcnt(0)\n"
        "v_mfma_f32_16x16x32_bf16 v[16:19], v[104:107], v[32:35], 0\n"
        "ds_read_b64_tr_b16 v[104:105], v4 offset:36864\n"
        "ds_read_b64_tr_b16 v[106:107], v4 offset:37376\n"
        "v_mfma_f32_16x16x32_bf16 v[20:23], v[108:111], v[32:35], 0\n"
        "ds_read_b64_tr_b16 v[108:109], v5 offset:36864\n"
        "ds_read_b64_tr_b16 v[110:111], v5 offset:37376\n"
        "v_mfma_f32_16x16x32_bf16 v[24:27], v[112:115], v[32:35], 0\n"
        "ds_read_b64_tr_b16 v[112:113], v6 offset:36864\n"
        "ds_read_b64_tr_b16 v[114:115], v6 offset:37376\n"
        "v_mfma_f32_16x16x32_bf16 v[28:31], v[116:119], v[32:35], 0\n"
        "ds_read_b64_tr_b16 v[116:117], v7 offset:36864\n"
        "ds_read_b64_tr_b16 v[118:119], v7 offset:37376\n"
        "v_mfma_f32_16x16x32_bf16 v[16:19], v[120:123], v[36:39], v[16:19]\n"
        "ds_read_b64_tr_b16 v[120:121], v4 offset:40960\n"
        "ds_read_b64_tr_b16 v[122:123], v4 offset:41472\n"
        "v_mfma_f32_16x16x32_bf16 v[20:23], v[124:127], v[36:39], v[20:23]\n"
        "ds_read_b64_tr_b16 v[124:125], v5 offset:40960\n"
        "ds_read_b64_tr_b16 v[126:127], v5 offset:41472\n"
        "v_mfma_f32_16x16x32_bf16 v[24:27], v[128:131], v[36:39], v[24:27]\n"
        "ds_read_b64_tr_b16 v[128:129], v6 offset:40960\n"
        "ds_read_b64_tr_b16 v[130:131], v6 offset:41472\n"
        "v_mfma_f32_16x16x32_bf16 v[28:31], v[132:135], v[36:39], v[28:31]\n"
        "ds_read_b64_tr_b16 v[132:133], v7 offset:40960\n"
        "ds_read_b64_tr_b16 v[134:135], v7 offset:41472\n"
        "v_mfma_f32_16x16x32_bf16 v[16:19], v[136:139], v[40:43], v[16:19]\n"
        "ds_read_b64_tr_b16 v[136:137], v4 offset:45056\n"
        "ds_read_b64_tr_b16 v[138:139], v4 offset:45568\n"
        "v_mfma_f32_16x16x32_bf16 v[20:23], v[140:143], v[40:43], v[20:23]\n"
        "ds_read_b64_tr_b16 v[140:141], v5 offset:45056\n"
        "ds_read_b64_tr_b16 v[142:143], v5 offset:45568\n"
        "v_mfma_f32_16x16x32_bf16 v[24:27], v[144:147], v[40:43], v[24:27]\n"
        "ds_read_b64_tr_b16 v[144:145], v6 offset:45056\n"
        "ds_read_b64_tr_b16 v[146:147], v6 offset:45568\n"
        "v_mfma_f32_16x16x32_bf16 v[28:31], v[148:151], v[40:43], v[28:31]\n"
        "ds_read_b64_tr_b16 v[148:149], v7 offset:45056\n"
        "ds_read_b64_tr_b16 v[150:151], v7 offset:45568\n"
        "v_mfma_f32_16x16x32_bf16 v[16:19], v[152:155], v[44:47], v[16:19]\n"
        "ds_read_b64_tr_b16 v[152:153], v4 offset:49152\n"
        "ds_read_b64_tr_b16 v[154:155], v4 offset:49664\n"
        "v_mfma_f32_16x16x32_bf16 v[20:23], v[156:159], v[44:47], v[20:23]\n"
        "ds_read_b64_tr_b16 v[156:157], v5 offset:49152\n"
        "ds_read_b64_tr_b16 v[158:159], v5 offset:49664\n"
        "v_mfma_f32_16x16x32_bf16 v[24:27], v[160:163], v[44:47], v[24:27]\n"
        "ds_read_b64_tr_b16 v[160:161], v6 offset:49152\n"
        "ds_read_b64_tr_b16 v[162:163], v6 offset:49664\n"
        "v_mfma_f32_16x16x32_bf16 v[28:31], v[164:167], v[44:47], v[28:31]\n"
        "ds_read_b64_tr_b16 v[164:165], v7 offset:49152\n"
        "ds_read_b64_tr_b16 v[166:167], v7 offset:49664\n"
        "v_mfma_f32_16x16x32_bf16 v[16:19], v[168:171], v[48:51], v[16:19]\n"
        "ds_read_b64_tr_b16 v[168:169], v4 offset:53248\n"
        "ds_read_b64_tr_b16 v[170:171], v4 offset:53760\n"
        "v_mfma_f32_16x16x32_bf16 v[20:23], v[172:175], v[48:51], v[20:23]\n"
        "ds_read_b64_tr_b16 v[172:173], v5 offset:53248\n"
        "ds_read_b64_tr_b16 v[174:175], v5 offset:53760\n"
        "v_mfma_f32_16x16x32_bf16 v[24:27], v[176:179], v[48:51], v[24:27]\n"
        "ds_read_b64_tr_b16 v[176:177], v6 offset:53248\n"
        "ds_read_b64_tr_b16 v[178:179], v6 offset:53760\n"
        "v_mfma_f32_16x16x32_bf16 v[28:31], v[180:183], v[48:51], v[28:31]\n"
        "ds_read_b64_tr_b16 v[180:181], v7 offset:53248\n"
        "ds_read_b64_tr_b16 v[182:183], v7 offset:53760\n"
        "s_waitcnt lgkmcnt(8)\n"
        "v_mfma_f32_16x16x32_bf16 v[16:19], v[184:187], v[52:55], v[16:19]\n"
        "v_mfma_f32_16x16x32_bf16 v[20:23], v[188:191], v[52:55], v[20:23]\n"
        "v_mfma_f32_16x16x32_bf16 v[24:27], v[192:195], v[52:55], v[24:27]\n"
        "v_mfma_f32_16x16x32_bf16 v[28:31], v[196:199], v[52:55], v[28:31]\n"
        "v_mfma_f32_16x16x32_bf16 v[16:19], v[200:203], v[56:59], v[16:19]\n"
        "v_mfma_f32_16x16x32_bf16 v[20:23], v[204:207], v[56:59], v[20:23]\n"
        "v_mfma_f32_16x16x32_bf16 v[24:27], v[208:211], v[56:59], v[24:27]\n"
        "v_mfma_f32_16x16x32_bf16 v[28:31], v[212:215], v[56:59], v[28:31]\n"
        "v_mfma_f32_16x16x32_bf16 v[16:19], v[216:219], v[60:63], v[16:19]\n"
        "v_mfma_f32_16x16x32_bf16 v[20:23], v[220:223], v[60:63], v[20:23]\n"
        "v_mfma_f32_16x16x32_bf16 v[24:27], v[224:227], v[60:63], v[24:27]\n"
        "v_mfma_f32_16x16x32_bf16 v[28:31], v[228:231], v[60:63], v[28:31]\n"
        "v_mfma_f32_16x16x32_bf16 v[16:19], v[232:235], v[64:67], v[16:19]\n"
        "v_mfma_f32_16x16x32_bf16 v[20:23], v[236:239], v[64:67], v[20:23]\n"
        "v_mfma_f32_16x16x32_bf16 v[24:27], v[240:243], v[64:67], v[24:27]\n"
        "v_mfma_f32_16x16x32_bf16 v[28:31], v[244:247], v[64:67], v[28:31]\n"
        "s_waitcnt lgkmcnt(0)\n"
        "v_mfma_f32_16x16x32_bf16 v[16:19], v[104:107], v[68:71], v[16:19]\n"
        "v_mfma_f32_16x16x32_bf16 v[20:23], v[108:111], v[68:71], v[20:23]\n"
        "v_mfma_f32_16x16x32_bf16 v[24:27], v[112:115], v[68:71], v[24:27]\n"
        "v_mfma_f32_16x16x32_bf16 v[28:31], v[116:119], v[68:71], v[28:31]\n"
        "v_mfma_f32_16x16x32_bf16 v[16:19], v[120:123], v[72:75], v[16:19]\n"
        "v_mfma_f32_16x16x32_bf16 v[20:23], v[124:127], v[72:75], v[20:23]\n"
        "v_mfma_f32_16x16x32_bf16 v[24:27], v[128:131], v[72:75], v[24:27]\n"
        "v_mfma_f32_16x16x32_bf16 v[28:31], v[132:135], v[72:75], v[28:31]\n"
        "v_mfma_f32_16x16x32_bf16 v[16:19], v[136:139], v[76:79], v[16:19]\n"
        "v_mfma_f32_16x16x32_bf16 v[20:23], v[140:143], v[76:79], v[20:23]\n"
        "v_mfma_f32_16x16x32_bf16 v[24:27], v[144:147], v[76:79], v[24:27]\n"
        "v_mfma_f32_16x16x32_bf16 v[28:31], v[148:151], v[76:79], v[28:31]\n"
        "v_mfma_f32_16x16x32_bf16 v[16:19], v[152:155], v[80:83], v[16:19]\n"
        "v_mfma_f32_16x16x32_bf16 v[20:23], v[156:159], v[80:83], v[20:23]\n"
        "v_mfma_f32_16x16x32_bf16 v[24:27], v[160:163], v[80:83], v[24:27]\n"
        "v_mfma_f32_16x16x32_bf16 v[28:31], v[164:167], v[80:83], v[28:31]\n"
        "v_mfma_f32_16x16x32_bf16 v[16:19], v[168:171], v[84:87], v[16:19]\n"
        "v_mfma_f32_16x16x32_bf16 v[20:23], v[172:175], v[84:87], v[20:23]\n"
        "v_mfma_f32_16x16x32_bf16 v[24:27], v[176:179], v[84:87], v[24:27]\n"
        "v_mfma_f32_16x16x32_bf16 v[28:31], v[180:183], v[84:87], v[28:31]\n"
        "s_nop 1\n"
        "s_branch L_st%=\n"
        ".fill 256, 4, 0xBF800000\n"
        :
        : [tid] "v"(threadIdx.x), [b] "s"(blockIdx.x), [ka] "s"(__builtin_amdgcn_kernarg_segment_ptr())
        : "memory", "scc", "vcc", "s8", "s9", "s10", "s11", "s12", "s13", "s14", "s15", "s16", "s17", "s18", "s19", "s20",
          "s21", "s22", "s23", "s24", "s25", "s26", "s27", "s28", "s29", "s30", "s31", "s33", "s34", "s35", "s36", "s37",
          "s38", "s39", "s40", "s41", "s42", "s43", "s44", "s45", "s46", "s47", "s48", "s49", "s50", "s51", "s52", "s53",
          "s54", "s55", "v1", "v2", "v3", "v4", "v5", "v6", "v7", "v8", "v9", "v10", "v11", "v12", "v13", "v14",
          "v15", "v16", "v17", "v18", "v19", "v20", "v21", "v22", "v23", "v24", "v25", "v26", "v27", "v28", "v29", "v30",
          "v31", "v32", "v33", "v34", "v35", "v36", "v37", "v38", "v39", "v40", "v41", "v42", "v43", "v44", "v45", "v46",
          "v47", "v48", "v49", "v50", "v51", "v52", "v53", "v54", "v55", "v56", "v57", "v58", "v59", "v60", "v61", "v62",
          "v63", "v64", "v65", "v66", "v67", "v68", "v69", "v70", "v71", "v72", "v73", "v74", "v75", "v76", "v77", "v78",
          "v79", "v80", "v81", "v82", "v83", "v84", "v85", "v86", "v87", "v88", "v89", "v90", "v91", "v92", "v93", "v94",
          "v95", "v96", "v97", "v98", "v99", "v100", "v101", "v102", "v103", "v104", "v105", "v106", "v107", "v108", "v109", "v110",
          "v111", "v112", "v113", "v114", "v115", "v116", "v117", "v118", "v119", "v120", "v121", "v122", "v123", "v124", "v125", "v126",
          "v127", "v128", "v129", "v130", "v131", "v132", "v133", "v134", "v135", "v136", "v137", "v138", "v139", "v140", "v141", "v142",
          "v143", "v144", "v145", "v146", "v147", "v148", "v149", "v150", "v151", "v152", "v153", "v154", "v155", "v156", "v157", "v158",
          "v159", "v160", "v161", "v162", "v163", "v164", "v165", "v166", "v167", "v168", "v169", "v170", "v171", "v172", "v173", "v174",
          "v175", "v176", "v177", "v178", "v179", "v180", "v181", "v182", "v183", "v184", "v185", "v186", "v187", "v188", "v189", "v190",
          "v191", "v192", "v193", "v194", "v195", "v196", "v197", "v198", "v199", "v200", "v201", "v202", "v203", "v204", "v205", "v206",
          "v207", "v208", "v209", "v210", "v211", "v212", "v213", "v214", "v215", "v216", "v217", "v218", "v219", "v220", "v221", "v222",
          "v223", "v224", "v225", "v226", "v227", "v228", "v229", "v230", "v231", "v232", "v233", "v234", "v235", "v236", "v237", "v238",
          "v239", "v240", "v241", "v242", "v243", "v244", "v245", "v246", "v247", "v248", "v249", "v250", "v251", "v252", "v253", "v254",
          "v255");
}

extern "C" void kernel_launch(void* const* d_in, const int* in_sizes, int n_in, void* d_out, int out_size, void* d_ws,
                              size_t ws_size, hipStream_t stream) {
    (void)hipFuncSetAttribute((const void*)geglu_ffn_kernel, hipFuncAttributeMaxDynamicSharedMemorySize, LDS_BYTES);
    geglu_ffn_kernel<<<256, 512, LDS_BYTES, stream>>>((const char*)d_in[0], (const char*)d_in[1], (const char*)d_in[2],
                                                       (const char*)d_in[3], (char*)d_out, (char*)d_ws);
}
```
